```python
import torch

def _softmax(x, axis=-1):
    return torch.softmax(x, dim=axis)
def _gelu(x):
    return torch.nn.functional.gelu(x, approximate="tanh")
def _silu(x):
    return torch.nn.functional.silu(x)
def _sigmoid(x):
    return torch.sigmoid(x)
def _layernorm(x, axis=-1, eps=1e-5):
    m = x.mean(dim=axis, keepdim=True)
    v = x.var(dim=axis, keepdim=True, unbiased=False)
    return (x - m) / torch.sqrt(v + eps)
def _scatter_add(tgt, idx, src, axis=0):
    return tgt.clone().index_add_(axis, idx, src)
def _segment_sum(src, seg, n):
    out = torch.zeros((n,) + src.shape[1:], dtype=src.dtype)
    return out.index_add_(0, seg, src)
def _pad(x, pw):
    flat = tuple(v for pair in reversed(pw) for v in pair)
    return torch.nn.functional.pad(x, flat)

kernel_name = 'standalone__edm_precond__diffusion'

def setup_inputs():

    _w = (lambda a: (lambda t: t.to(torch.bfloat16) if t.is_floating_point() else t)(torch.as_tensor(a)))
    B, C, HW = 2, 320, 128
    return {
        "x": _w((lambda *s: torch.randn(*s, dtype=torch.bfloat16) * 0.2)(B, C, HW, HW)),
        "sigma": _w((lambda _x: torch.exp(torch.as_tensor(_x)))((lambda *s: torch.randn(*s, dtype=torch.bfloat16) * 3.2)(B))),
        "w_in": _w((lambda *s: torch.randn(*s, dtype=torch.bfloat16) * 3.2)(C, C) * (C ** -0.5)),
        "w_emb": _w((lambda *s: torch.randn(*s, dtype=torch.bfloat16) * 3.2)(1, C)),
        "w_out": _w((lambda *s: torch.randn(*s, dtype=torch.bfloat16) * 3.2)(C, C) * (C ** -0.5)),
    }

def reference(x, sigma, w_in, w_emb, w_out):
    B, C, H, W = x.shape
    sigma_data = 0.5
    s = sigma.reshape(B, 1, 1, 1)
    denom = s * s + sigma_data * sigma_data
    c_skip = (sigma_data * sigma_data) / denom
    c_out  = s * sigma_data / torch.sqrt(denom)
    c_in   = 1.0 / torch.sqrt(denom)
    c_noise = 0.25 * torch.log(sigma).reshape(B, 1)
    h = torch.einsum("bchw,cd->bdhw", c_in * x, w_in)
    h = h + (c_noise @ w_emb).reshape(B, C, 1, 1)
    h = _silu(h)
    Fx = torch.einsum("bchw,cd->bdhw", h, w_out)
    return c_skip * x + c_out * Fx
```

```cpp
#include <hip/hip_runtime.h>
#include <hip/hip_bf16.h>
typedef unsigned short u16;
__device__ __forceinline__ float bf2f(u16 v){ return __uint_as_float(((unsigned)v)<<16); }
__device__ __forceinline__ u16 f2bf(float f){ unsigned u=__float_as_uint(f); u += 0x7fffu + ((u>>16)&1u); return (u16)(u>>16);}
__device__ __forceinline__ float rb(float f){ return bf2f(f2bf(f)); }
__global__ void naive_k(const u16* x, const u16* sigma, const u16* w_in, const u16* w_emb, const u16* w_out, u16* out){
  __shared__ float xs[320][33];
  __shared__ float hs[320][33];
  int blk = blockIdx.x; int b = blk / 512; int p0 = (blk % 512) * 32;
  int t = threadIdx.x;
  float s = bf2f(sigma[b]);
  float denom = rb(rb(s*s) + 0.25f);
  float c_skip = rb(0.25f/denom);
  float sq = rb(sqrtf(denom));
  float c_out = rb(rb(s*0.5f)/sq);
  float c_in = rb(1.0f/sq);
  float c_noise = rb(0.25f*rb(logf(s)));
  const u16* xb = x + (size_t)b*320*16384 + p0;
  for (int p=0;p<32;p++) xs[t][p] = bf2f(xb[(size_t)t*16384+p]);
  __syncthreads();
  float emb = rb(c_noise*bf2f(w_emb[t]));
  float acc[32];
  for (int p=0;p<32;p++) acc[p]=0.f;
  for (int c=0;c<320;c++){ float w=bf2f(w_in[c*320+t]); for(int p=0;p<32;p++) acc[p]+=w*xs[c][p]; }
  for (int p=0;p<32;p++){ float h = c_in*acc[p]+emb; hs[t][p] = rb(h/(1.f+__expf(-h))); }
  __syncthreads();
  for (int p=0;p<32;p++) acc[p]=0.f;
  for (int c=0;c<320;c++){ float w=bf2f(w_out[c*320+t]); for(int p=0;p<32;p++) acc[p]+=w*hs[c][p]; }
  u16* ob = out + (size_t)b*320*16384 + p0;
  for (int p=0;p<32;p++) ob[(size_t)t*16384+p] = f2bf(c_skip*xs[t][p] + c_out*acc[p]);
}
extern "C" void kernel_launch(void* const* d_in, const int* in_sizes, int n_in, void* d_out, int out_size, void* d_ws, size_t ws_size, hipStream_t stream){
  naive_k<<<1024,320,0,stream>>>((const u16*)d_in[0],(const u16*)d_in[1],(const u16*)d_in[2],(const u16*)d_in[3],(const u16*)d_in[4],(u16*)d_out);
}
```

```cpp
#include <hip/hip_runtime.h>
#include <hip/hip_bf16.h>

__global__ __launch_bounds__(512, 2) void edm_precond_kernel(const void* x, const void* sigma, const void* w_in, const void* w_emb, const void* w_out, void* out) {
  __shared__ __attribute__((aligned(1024))) char lds_image[162816];
  auto ka = __builtin_amdgcn_kernarg_segment_ptr();
  unsigned blk = blockIdx.x; unsigned tid = threadIdx.x;
  asm volatile(
    ".Ledm_begin:\n"
    "  s_load_dwordx8 s[8:15], %0, 0\n"
    "  v_bfe_u32 v4, %2, 3, 4\n"
    "  v_lshrrev_b32 v9, 4, %2\n"
    "  v_and_b32 v5, 6, v4\n"
    "  v_xor_b32 v5, v5, %2\n"
    "  v_and_b32 v5, 7, v5\n"
    "  v_and_or_b32 v7, v9, 16, v4\n"
    "  v_and_or_b32 v8, v9, 8, v5\n"
    "  v_lshlrev_b32 v8, 4, v8\n"
    "  v_lshl_add_u32 v8, v7, 15, v8\n"
    "  s_lshr_b32 s28, %1, 7\n"
    "  s_lshl_b32 s29, %1, 25\n"
    "  s_mul_i32 s24, s28, 10485760\n"
    "  s_lshr_b32 s29, s29, 17\n"
    "  v_readfirstlane_b32 s20, %2\n"
    "  s_add_u32 s24, s24, s29\n"
    "  v_add_u32 v8, s24, v8\n"
    "  s_lshl4_add_u32 m0, s20, 71680\n"
    "  v_lshlrev_b32 v13, 2, %2\n"
    "  s_getpc_b64 s[52:53]\n"
    ".Ledm_pc0:\n"
    "  s_mov_b32 exec_lo, 1\n"
    "  s_mov_b32 exec_hi, 1\n"
    "  global_load_dword v42, v13, s[52:53] offset:128\n"
    "  s_add_u32 s30, s52, .Ledm_T1m-.Ledm_pc0\n"
    "  s_addc_u32 s31, s53, 0\n"
    "  global_load_dword v41, v13, s[30:31] offset:0\n"
    "  s_add_u32 s30, s52, .Ledm_Lm-.Ledm_pc0\n"
    "  s_addc_u32 s31, s53, 0\n"
    "  global_load_dword v43, v13, s[30:31] offset:0\n"
    "  s_lshr_b32 s23, %1, 3\n"
    "  s_mulk_i32 s23, 1088\n"
    "  v_add_u32 v16, s23, v13\n"
    "  v_min_u32 v16, (.Ledm_end-.Ledm_pc0-4)&~3, v16\n"
    "  global_load_dword v45, v16, s[52:53] offset:0\n"
    "  s_mov_b64 exec, -1\n"
    "  s_waitcnt lgkmcnt(0)\n"
    "  global_load_lds_dwordx4 v8, s[8:9] offset:0\n"
    "  v_lshrrev_b32 v15, 3, %2\n"
    "  s_lshr_b32 s23, %1, 3\n"
    "  v_min_u32 v15, 49, v15\n"
    "  s_mulk_i32 s23, 6400\n"
    "  v_lshl_add_u32 v15, v15, 7, s23\n"
    "  s_mov_b32 exec_lo, 0x01010101\n"
    "  s_mov_b32 exec_hi, exec_lo\n"
    "  global_load_dword v40, v15, s[12:13] offset:0\n"
    "  s_mov_b64 exec, -1\n"
    "  v_and_b32 v4, 7, v4\n"
    "  v_lshlrev_b32 v5, 4, v5\n"
    "  v_mul_u32_u24 v6, 0x280, v4\n"
    "  s_lshr_b32 s20, s20, 6\n"
    "  v_add_u32 v6, v6, v5\n"
    "  s_and_b32 s21, s20, 3\n"
    "  s_lshr_b32 s23, s20, 2\n"
    "  s_mul_i32 s26, s21, 5120\n"
    "  s_lshl_b32 s27, s23, 8\n"
    "  v_add_u32 v12, s26, v6\n"
    "  s_addk_i32 s27, 256\n"
    "  v_add_u32 v11, s27, v12\n"
    "  s_mul_i32 s27, s23, 384\n"
    "  v_add_u32 v10, s27, v12\n"
    "  v_add_u32 v12, 128, v12\n"
    "  s_and_b32 s27, s21, 1\n"
    "  s_lshl_b32 s27, s27, 12\n"
    "  s_sub_u32 s26, s26, s27\n"
    "  s_mulk_i32 s23, 6144\n"
    "  s_add_u32 m0, s26, s23\n"
    "  s_nop 0\n"
    "  global_load_lds_dwordx4 v10, s[12:13] offset:0\n"
    "  s_addk_i32 m0, 2048\n"
    "  s_cmp_lt_u32 s20, 4\n"
    "  global_load_lds_dwordx4 v11, s[12:13] offset:0\n"
    "  s_cbranch_scc0 .Ledm_p2\n"
    "  s_addk_i32 m0, 2048\n"
    "  s_nop 0\n"
    "  global_load_lds_dwordx4 v12, s[12:13] offset:0\n"
    ".Ledm_p2:\n"
    "  s_load_dwordx4 s[16:19], %0, 32\n"
    "  v_add_u32 v13, 153600, v13\n"
    "  v_mov_b32 v14, 0\n"
    "  ds_write_b32 v13, v14 offset:0\n"
    "  s_lshr_b32 s26, s21, 1\n"
    "  s_mul_i32 s26, s26, 0xb0000002\n"
    "  s_and_b32 s23, s21, 1\n"
    "  s_mul_i32 s23, s23, 1\n"
    "  s_add_u32 s26, s26, s23\n"
    "  v_mov_b32 v15, s26\n"
    "  v_and_b32 v13, 0x3ff, v13\n"
    "  v_add_u32 v13, 153600, v13\n"
    "  ds_write_b32 v13, v15 offset:2048\n"
    "  s_add_u32 s52, s52, .Ledm_tab-.Ledm_pc0\n"
    "  s_addc_u32 s53, s53, 0\n"
    "  s_lshl2_add_u32 m0, s20, 155648\n"
    "  v_mov_b32 v14, 4\n"
    "  s_waitcnt lgkmcnt(0)\n"
    "  s_barrier\n"
    "  s_mov_b64 exec, 1\n"
    "  global_load_lds_dword v14, s[52:53]\n"
    "  s_mov_b64 exec, -1\n"
    "  s_load_dword s38, s[10:11], 0\n"
    "  v_and_b32 v3, 63, %2\n"
    "  s_mov_b32 s22, %1\n"
    "  v_lshl_add_u32 v7, v4, 15, v5\n"
    "  v_add_u32 v7, s24, v7\n"
    "  s_mov_b32 s39, 524288\n"
    "  s_cmp_lt_u32 s20, 4\n"
    "  s_cbranch_scc1 .Ledm_T1m\n"
    "  s_cmp_lt_u32 s20, 7\n"
    "  s_cbranch_scc1 .Ledm_E\n"
    "  v_add_u32 v10, 3072, v6\n"
    "  v_add_u32 v11, 7168, v6\n"
    "  v_add_u32 v12, 1280, v6\n"
    "  v_add_u32 v13, 5376, v6\n"
    "  v_add_u32 v14, 3200, v6\n"
    "  v_add_u32 v15, 7296, v6\n"
    "  v_add_u32 v16, 1408, v6\n"
    "  v_add_u32 v17, 5504, v6\n"
    "  v_add_u32 v18, 3584, v6\n"
    "  v_add_u32 v19, 7680, v6\n"
    "  v_lshlrev_b32 v8, 4, v3\n"
    "  s_mov_b32 m0, 156672\n"
    "  v_add_u32 v30, 3072, v7\n"
    "  v_add_u32 v31, 264192, v7\n"
    "  v_add_u32 v32, 1152, v7\n"
    "  v_add_u32 v33, 262272, v7\n"
    "  v_add_u32 v34, 527360, v7\n"
    "  v_add_u32 v35, 788480, v7\n"
    "  v_add_u32 v36, 525440, v7\n"
    "  v_add_u32 v37, 786560, v7\n"
    "  s_sub_u32 s44, s8, 3072\n"
    "  s_subb_u32 s45, s9, 0\n"
    "  s_add_u32 s30, s12, 7168\n"
    "  s_addc_u32 s31, s13, 0\n"
    "  s_mov_b32 exec_hi, 0xff\n"
    "  global_load_lds_dwordx4 v8, s[14:15] offset:0\n"
    "  s_mov_b32 exec_hi, -1\n"
    "  s_add_u32 s30, s30, 10240\n"
    "  s_addc_u32 s31, s31, 0\n"
    "  s_mov_b32 m0, 20480\n"
    "  s_nop 0\n"
    "  global_load_lds_dwordx4 v10, s[30:31] offset:0\n"
    "  global_load_lds_dwordx4 v11, s[30:31] offset:1024\n"
    "  global_load_lds_dwordx4 v12, s[30:31] offset:2048\n"
    "  global_load_lds_dwordx4 v13, s[30:31] offset:3072\n"
    "  s_mov_b32 m0, 24576\n"
    "  s_nop 0\n"
    "  global_load_lds_dwordx4 v14, s[30:31] offset:0\n"
    "  global_load_lds_dwordx4 v15, s[30:31] offset:1024\n"
    "  global_load_lds_dwordx4 v16, s[30:31] offset:2048\n"
    "  global_load_lds_dwordx4 v17, s[30:31] offset:3072\n"
    "  s_mov_b32 m0, 28672\n"
    "  s_nop 0\n"
    "  global_load_lds_dwordx4 v18, s[30:31] offset:0\n"
    "  global_load_lds_dwordx4 v19, s[30:31] offset:1024\n"
    "  v_mov_b32 v38, 12\n"
    "  s_mov_b32 m0, 156160\n"
    "  s_nop 0\n"
    "  global_load_lds_dword v38, s[52:53]\n"
    "  s_add_u32 s44, s44, 1048576\n"
    "  s_addc_u32 s45, s45, 0\n"
    "  s_mov_b32 m0, 79872\n"
    "  s_nop 0\n"
    "  global_load_lds_dwordx4 v30, s[44:45] offset:0\n"
    "  global_load_lds_dwordx4 v31, s[44:45] offset:1024\n"
    "  global_load_lds_dwordx4 v32, s[44:45] offset:2048\n"
    "  global_load_lds_dwordx4 v33, s[44:45] offset:3072\n"
    "  s_mov_b32 m0, 83968\n"
    "  s_nop 0\n"
    "  global_load_lds_dwordx4 v34, s[44:45] offset:0\n"
    "  global_load_lds_dwordx4 v35, s[44:45] offset:1024\n"
    "  global_load_lds_dwordx4 v36, s[44:45] offset:2048\n"
    "  global_load_lds_dwordx4 v37, s[44:45] offset:3072\n"
    "  v_mov_b32 v38, 20\n"
    "  s_mov_b32 m0, 156416\n"
    "  s_nop 0\n"
    "  global_load_lds_dword v38, s[52:53]\n"
    "  s_add_u32 s30, s30, 10240\n"
    "  s_addc_u32 s31, s31, 0\n"
    "  s_mov_b32 m0, 30720\n"
    "  s_nop 0\n"
    "  global_load_lds_dwordx4 v10, s[30:31] offset:0\n"
    "  global_load_lds_dwordx4 v11, s[30:31] offset:1024\n"
    "  global_load_lds_dwordx4 v12, s[30:31] offset:2048\n"
    "  global_load_lds_dwordx4 v13, s[30:31] offset:3072\n"
    "  s_mov_b32 m0, 34816\n"
    "  s_nop 0\n"
    "  global_load_lds_dwordx4 v14, s[30:31] offset:0\n"
    "  global_load_lds_dwordx4 v15, s[30:31] offset:1024\n"
    "  global_load_lds_dwordx4 v16, s[30:31] offset:2048\n"
    "  global_load_lds_dwordx4 v17, s[30:31] offset:3072\n"
    "  s_mov_b32 m0, 38912\n"
    "  s_nop 0\n"
    "  global_load_lds_dwordx4 v18, s[30:31] offset:0\n"
    "  global_load_lds_dwordx4 v19, s[30:31] offset:1024\n"
    "  v_mov_b32 v38, 16\n"
    "  s_mov_b32 m0, 156160\n"
    "  s_nop 0\n"
    "  global_load_lds_dword v38, s[52:53]\n"
    "  s_branch .Ledm_Lm\n"
    ".Ledm_tab:\n"
    "  s_movk_i32 s0, 0\n"
    "  s_movk_i32 s0, 1\n"
    "  s_movk_i32 s0, 2\n"
    "  s_movk_i32 s0, 3\n"
    "  s_movk_i32 s0, 4\n"
    "  s_movk_i32 s0, 5\n"
    "  s_movk_i32 s0, 6\n"
    "  s_movk_i32 s0, 7\n"
    "  s_movk_i32 s0, 8\n"
    "  s_movk_i32 s0, 9\n"
    "  s_movk_i32 s0, 10\n"
    "  s_movk_i32 s0, 11\n"
    "  s_movk_i32 s0, 12\n"
    "  s_movk_i32 s0, 13\n"
    "  s_movk_i32 s0, 14\n"
    "  s_movk_i32 s0, 15\n"
    "  s_movk_i32 s0, 16\n"
    "  s_movk_i32 s0, 17\n"
    "  s_movk_i32 s0, 18\n"
    "  s_movk_i32 s0, 19\n"
    "  s_movk_i32 s0, 20\n"
    "  s_movk_i32 s0, 21\n"
    ".Ledm_E:\n"
    "  s_lshr_b32 s26, s22, 3\n"
    "  s_cmp_eq_u32 s21, 2\n"
    "  s_cbranch_scc1 .Ledm_Ec\n"
    "  s_mul_i32 s27, s26, 6400\n"
    "  s_mul_i32 s23, s21, 3200\n"
    "  s_add_u32 s27, s27, s23\n"
    "  v_min_u32 v12, 24, v3\n"
    "  v_lshlrev_b32 v12, 7, v12\n"
    "  v_add_u32 v12, s27, v12\n"
    "  global_load_dword v44, v12, s[16:17] offset:0\n"
    "  s_branch .Ledm_Em\n"
    ".Ledm_Ec:\n"
    "  s_branch .Ledm_Em\n"
    ".Ledm_T1m:\n"
    "  s_setprio 1\n"
    "  v_lshrrev_b32 v96, 2, v3\n"
    "  v_lshlrev_b32 v98, 7, v96\n"
    "  v_bfe_u32 v97, v3, 3, 2\n"
    "  v_lshl_or_b32 v98, v97, 5, v98\n"
    "  v_and_b32 v97, 3, v3\n"
    "  v_lshl_or_b32 v98, v97, 3, v98\n"
    "  v_xor_b32 v4, 0, v98\n"
    "  v_add_u32 v4, 71680, v4\n"
    "  v_xor_b32 v5, 32, v98\n"
    "  v_add_u32 v5, 71680, v5\n"
    "  v_xor_b32 v6, 64, v98\n"
    "  v_add_u32 v6, 71680, v6\n"
    "  v_xor_b32 v7, 96, v98\n"
    "  v_add_u32 v7, 71680, v7\n"
    "  s_and_b32 s23, s21, 1\n"
    "  s_lshl_b32 s23, s23, 6\n"
    "  v_xor_b32 v2, s23, v98\n"
    "  s_lshr_b32 s26, s21, 1\n"
    "  s_lshl_b32 s27, s26, 12\n"
    "  v_add_u32 v2, s27, v2\n"
    "  v_xor_b32 v13, 32, v2\n"
    "  s_lshl_b32 s23, s21, 5\n"
    "  v_xor_b32 v14, s23, v98\n"
    "  v_add_u32 v14, 8192, v14\n"
    "  v_lshlrev_b32 v96, 4, v3\n"
    "  s_lshl_b32 s23, s21, 11\n"
    "  v_add_u32 v8, s23, v96\n"
    "  s_lshl_b32 s26, s26, 10\n"
    "  s_and_b32 s23, s21, 1\n"
    "  s_lshl_b32 s23, s23, 3\n"
    "  s_add_u32 s23, s23, s26\n"
    "  s_add_u32 s23, s23, 8192\n"
    "  v_add_u32 v9, s23, v96\n"
    "  s_lshl_b32 s23, s21, 8\n"
    "  s_add_u32 s23, s23, 153600\n"
    "  v_lshl_add_u32 v10, v3, 2, s23\n"
    "  v_and_b32 v96, 1, v3\n"
    "  v_mul_u32_u24 v96, 0x104, v96\n"
    "  v_add_u32 v15, 156168, v96\n"
    "  v_mov_b32 v12, 2\n"
    "  v_and_b32 v96, 7, v3\n"
    "  v_lshlrev_b32 v96, 2, v96\n"
    "  v_add_u32 v96, 155648, v96\n"
    "  s_waitcnt vmcnt(0)\n"
    ".Ledm_pl35:\n"
    "  s_sub_u32 s39, s39, 1\n"
    "  s_cbranch_scc1 .Ledm_abort\n"
    "  ds_read_b32 v11, v96 offset:0\n"
    "  s_waitcnt lgkmcnt(0)\n"
    "  v_cmp_gt_u32_e32 vcc, 0xb0000001, v11\n"
    "  s_cbranch_vccnz .Ledm_pl35\n"
    "  ds_read_b64_tr_b16 v[16:17], v2 offset:0\n"
    "  ds_read_b64_tr_b16 v[18:19], v2 offset:10240\n"
    "  ds_read_b64_tr_b16 v[56:57], v4 offset:0\n"
    "  ds_read_b64_tr_b16 v[58:59], v4 offset:4096\n"
    "  ds_read_b64_tr_b16 v[20:21], v13 offset:0\n"
    "  ds_read_b64_tr_b16 v[22:23], v13 offset:10240\n"
    "  ds_read_b64_tr_b16 v[24:25], v2 offset:2048\n"
    "  ds_read_b64_tr_b16 v[26:27], v2 offset:12288\n"
    "  ds_read_b64_tr_b16 v[28:29], v13 offset:2048\n"
    "  ds_read_b64_tr_b16 v[30:31], v13 offset:12288\n"
    "  ds_read_b64_tr_b16 v[32:33], v14 offset:0\n"
    "  ds_read_b64_tr_b16 v[34:35], v14 offset:10240\n"
    "  ds_read_b64_tr_b16 v[60:61], v5 offset:0\n"
    "  ds_read_b64_tr_b16 v[62:63], v5 offset:4096\n"
    "  ds_read_b64_tr_b16 v[64:65], v6 offset:0\n"
    "  s_waitcnt lgkmcnt(14)\n"
    "  ds_read_b64_tr_b16 v[66:67], v6 offset:4096\n"
    "  s_waitcnt lgkmcnt(12)\n"
    "  v_mfma_f32_16x16x32_bf16 v[96:99], v[16:19], v[56:59], 0\n"
    "  ds_read_b64_tr_b16 v[68:69], v7 offset:0\n"
    "  s_waitcnt lgkmcnt(11)\n"
    "  v_mfma_f32_16x16x32_bf16 v[100:103], v[20:23], v[56:59], 0\n"
    "  ds_read_b64_tr_b16 v[70:71], v7 offset:4096\n"
    "  s_waitcnt lgkmcnt(10)\n"
    "  v_mfma_f32_16x16x32_bf16 v[104:107], v[24:27], v[56:59], 0\n"
    "  ds_add_u32 v10, v12 offset:0\n"
    "  s_waitcnt lgkmcnt(9)\n"
    "  v_mfma_f32_16x16x32_bf16 v[108:111], v[28:31], v[56:59], 0\n"
    "  s_waitcnt lgkmcnt(7)\n"
    "  v_mfma_f32_16x16x32_bf16 v[112:115], v[32:35], v[56:59], 0\n"
    "  s_waitcnt lgkmcnt(5)\n"
    "  v_mfma_f32_16x16x32_bf16 v[116:119], v[16:19], v[60:63], 0\n"
    "  ds_read_b64_tr_b16 v[56:57], v4 offset:2048\n"
    "  v_mfma_f32_16x16x32_bf16 v[120:123], v[20:23], v[60:63], 0\n"
    "  ds_read_b64_tr_b16 v[58:59], v4 offset:6144\n"
    "  v_mfma_f32_16x16x32_bf16 v[124:127], v[24:27], v[60:63], 0\n"
    "  v_mfma_f32_16x16x32_bf16 v[128:131], v[28:31], v[60:63], 0\n"
    "  v_mfma_f32_16x16x32_bf16 v[132:135], v[32:35], v[60:63], 0\n"
    "  s_waitcnt lgkmcnt(5)\n"
    "  v_mfma_f32_16x16x32_bf16 v[136:139], v[16:19], v[64:67], 0\n"
    "  ds_read_b64_tr_b16 v[60:61], v5 offset:2048\n"
    "  v_mfma_f32_16x16x32_bf16 v[140:143], v[20:23], v[64:67], 0\n"
    "  ds_read_b64_tr_b16 v[62:63], v5 offset:6144\n"
    "  v_mfma_f32_16x16x32_bf16 v[144:147], v[24:27], v[64:67], 0\n"
    "  v_mfma_f32_16x16x32_bf16 v[148:151], v[28:31], v[64:67], 0\n"
    "  v_mfma_f32_16x16x32_bf16 v[152:155], v[32:35], v[64:67], 0\n"
    "  s_waitcnt lgkmcnt(5)\n"
    "  v_mfma_f32_16x16x32_bf16 v[156:159], v[16:19], v[68:71], 0\n"
    "  ds_read_b64_tr_b16 v[64:65], v6 offset:2048\n"
    "  v_mfma_f32_16x16x32_bf16 v[160:163], v[20:23], v[68:71], 0\n"
    "  ds_read_b64_tr_b16 v[66:67], v6 offset:6144\n"
    "  v_mfma_f32_16x16x32_bf16 v[164:167], v[24:27], v[68:71], 0\n"
    "  v_mfma_f32_16x16x32_bf16 v[168:171], v[28:31], v[68:71], 0\n"
    "  v_mfma_f32_16x16x32_bf16 v[172:175], v[32:35], v[68:71], 0\n"
    "  ds_read_b32 v11, v15 offset:0\n"
    "  s_waitcnt lgkmcnt(5)\n"
    "  v_mfma_f32_16x16x32_bf16 v[176:179], v[16:19], v[56:59], 0\n"
    "  ds_read_b64_tr_b16 v[68:69], v7 offset:2048\n"
    "  v_mfma_f32_16x16x32_bf16 v[180:183], v[20:23], v[56:59], 0\n"
    "  ds_read_b64_tr_b16 v[70:71], v7 offset:6144\n"
    "  v_mfma_f32_16x16x32_bf16 v[184:187], v[24:27], v[56:59], 0\n"
    "  v_mfma_f32_16x16x32_bf16 v[188:191], v[28:31], v[56:59], 0\n"
    "  s_waitcnt lgkmcnt(2)\n"
    "  v_cmp_gt_u32_e32 vcc, 0xb0000004, v11\n"
    "  s_cbranch_vccnz .Ledm_sl36\n"
    ".Ledm_bk37:\n"
    "  v_mfma_f32_16x16x32_bf16 v[192:195], v[32:35], v[56:59], 0\n"
    "  ds_read_b64_tr_b16 v[56:57], v4 offset:8192\n"
    "  v_mfma_f32_16x16x32_bf16 v[196:199], v[16:19], v[60:63], 0\n"
    "  ds_read_b64_tr_b16 v[58:59], v4 offset:12288\n"
    "  v_mfma_f32_16x16x32_bf16 v[200:203], v[20:23], v[60:63], 0\n"
    "  ds_read_b64_tr_b16 v[36:37], v2 offset:20480\n"
    "  v_mfma_f32_16x16x32_bf16 v[204:207], v[24:27], v[60:63], 0\n"
    "  ds_read_b64_tr_b16 v[38:39], v2 offset:30720\n"
    "  v_mfma_f32_16x16x32_bf16 v[208:211], v[28:31], v[60:63], 0\n"
    "  ds_read_b64_tr_b16 v[40:41], v13 offset:20480\n"
    "  v_mfma_f32_16x16x32_bf16 v[212:215], v[32:35], v[60:63], 0\n"
    "  ds_read_b64_tr_b16 v[42:43], v13 offset:30720\n"
    "  v_mfma_f32_16x16x32_bf16 v[216:219], v[16:19], v[64:67], 0\n"
    "  ds_read_b64_tr_b16 v[44:45], v2 offset:22528\n"
    "  v_mfma_f32_16x16x32_bf16 v[220:223], v[20:23], v[64:67], 0\n"
    "  ds_read_b64_tr_b16 v[46:47], v2 offset:32768\n"
    "  v_mfma_f32_16x16x32_bf16 v[224:227], v[24:27], v[64:67], 0\n"
    "  ds_read_b64_tr_b16 v[48:49], v13 offset:22528\n"
    "  v_mfma_f32_16x16x32_bf16 v[228:231], v[28:31], v[64:67], 0\n"
    "  ds_read_b64_tr_b16 v[50:51], v13 offset:32768\n"
    "  v_mfma_f32_16x16x32_bf16 v[232:235], v[32:35], v[64:67], 0\n"
    "  ds_read_b64_tr_b16 v[52:53], v14 offset:20480\n"
    "  s_waitcnt lgkmcnt(11)\n"
    "  v_mfma_f32_16x16x32_bf16 v[236:239], v[16:19], v[68:71], 0\n"
    "  ds_read_b64_tr_b16 v[54:55], v14 offset:30720\n"
    "  v_mfma_f32_16x16x32_bf16 v[240:243], v[20:23], v[68:71], 0\n"
    "  ds_read_b64_tr_b16 v[60:61], v5 offset:8192\n"
    "  v_mfma_f32_16x16x32_bf16 v[244:247], v[24:27], v[68:71], 0\n"
    "  ds_read_b64_tr_b16 v[62:63], v5 offset:12288\n"
    "  v_mfma_f32_16x16x32_bf16 v[248:251], v[28:31], v[68:71], 0\n"
    "  ds_read_b64_tr_b16 v[64:65], v6 offset:8192\n"
    "  v_mfma_f32_16x16x32_bf16 v[252:255], v[32:35], v[68:71], 0\n"
    "  s_waitcnt lgkmcnt(14)\n"
    "  ds_read_b64_tr_b16 v[66:67], v6 offset:12288\n"
    "  s_waitcnt lgkmcnt(12)\n"
    "  v_mfma_f32_16x16x32_bf16 v[96:99], v[36:39], v[56:59], v[96:99]\n"
    "  ds_read_b64_tr_b16 v[68:69], v7 offset:8192\n"
    "  s_waitcnt lgkmcnt(11)\n"
    "  v_mfma_f32_16x16x32_bf16 v[100:103], v[40:43], v[56:59], v[100:103]\n"
    "  ds_read_b64_tr_b16 v[70:71], v7 offset:12288\n"
    "  s_waitcnt lgkmcnt(10)\n"
    "  v_mfma_f32_16x16x32_bf16 v[104:107], v[44:47], v[56:59], v[104:107]\n"
    "  ds_add_u32 v10, v12 offset:0\n"
    "  s_waitcnt lgkmcnt(9)\n"
    "  v_mfma_f32_16x16x32_bf16 v[108:111], v[48:51], v[56:59], v[108:111]\n"
    "  s_waitcnt lgkmcnt(7)\n"
    "  v_mfma_f32_16x16x32_bf16 v[112:115], v[52:55], v[56:59], v[112:115]\n"
    "  s_waitcnt lgkmcnt(5)\n"
    "  v_mfma_f32_16x16x32_bf16 v[116:119], v[36:39], v[60:63], v[116:119]\n"
    "  ds_read_b64_tr_b16 v[56:57], v4 offset:10240\n"
    "  v_mfma_f32_16x16x32_bf16 v[120:123], v[40:43], v[60:63], v[120:123]\n"
    "  ds_read_b64_tr_b16 v[58:59], v4 offset:14336\n"
    "  v_mfma_f32_16x16x32_bf16 v[124:127], v[44:47], v[60:63], v[124:127]\n"
    "  v_mfma_f32_16x16x32_bf16 v[128:131], v[48:51], v[60:63], v[128:131]\n"
    "  v_mfma_f32_16x16x32_bf16 v[132:135], v[52:55], v[60:63], v[132:135]\n"
    "  s_waitcnt lgkmcnt(5)\n"
    "  v_mfma_f32_16x16x32_bf16 v[136:139], v[36:39], v[64:67], v[136:139]\n"
    "  ds_read_b64_tr_b16 v[60:61], v5 offset:10240\n"
    "  v_mfma_f32_16x16x32_bf16 v[140:143], v[40:43], v[64:67], v[140:143]\n"
    "  ds_read_b64_tr_b16 v[62:63], v5 offset:14336\n"
    "  v_mfma_f32_16x16x32_bf16 v[144:147], v[44:47], v[64:67], v[144:147]\n"
    "  v_mfma_f32_16x16x32_bf16 v[148:151], v[48:51], v[64:67], v[148:151]\n"
    "  v_mfma_f32_16x16x32_bf16 v[152:155], v[52:55], v[64:67], v[152:155]\n"
    "  s_waitcnt lgkmcnt(5)\n"
    "  v_mfma_f32_16x16x32_bf16 v[156:159], v[36:39], v[68:71], v[156:159]\n"
    "  ds_read_b64_tr_b16 v[64:65], v6 offset:10240\n"
    "  v_mfma_f32_16x16x32_bf16 v[160:163], v[40:43], v[68:71], v[160:163]\n"
    "  ds_read_b64_tr_b16 v[66:67], v6 offset:14336\n"
    "  v_mfma_f32_16x16x32_bf16 v[164:167], v[44:47], v[68:71], v[164:167]\n"
    "  v_mfma_f32_16x16x32_bf16 v[168:171], v[48:51], v[68:71], v[168:171]\n"
    "  v_mfma_f32_16x16x32_bf16 v[172:175], v[52:55], v[68:71], v[172:175]\n"
    "  ds_read_b32 v11, v15 offset:0\n"
    "  s_waitcnt lgkmcnt(5)\n"
    "  v_mfma_f32_16x16x32_bf16 v[176:179], v[36:39], v[56:59], v[176:179]\n"
    "  ds_read_b64_tr_b16 v[68:69], v7 offset:10240\n"
    "  v_mfma_f32_16x16x32_bf16 v[180:183], v[40:43], v[56:59], v[180:183]\n"
    "  ds_read_b64_tr_b16 v[70:71], v7 offset:14336\n"
    "  v_mfma_f32_16x16x32_bf16 v[184:187], v[44:47], v[56:59], v[184:187]\n"
    "  v_mfma_f32_16x16x32_bf16 v[188:191], v[48:51], v[56:59], v[188:191]\n"
    "  s_waitcnt lgkmcnt(2)\n"
    "  v_cmp_gt_u32_e32 vcc, 0xb0000006, v11\n"
    "  s_cbranch_vccnz .Ledm_sl38\n"
    ".Ledm_bk39:\n"
    "  v_mfma_f32_16x16x32_bf16 v[192:195], v[52:55], v[56:59], v[192:195]\n"
    "  ds_read_b64_tr_b16 v[56:57], v4 offset:16384\n"
    "  v_mfma_f32_16x16x32_bf16 v[196:199], v[36:39], v[60:63], v[196:199]\n"
    "  ds_read_b64_tr_b16 v[58:59], v4 offset:20480\n"
    "  v_mfma_f32_16x16x32_bf16 v[200:203], v[40:43], v[60:63], v[200:203]\n"
    "  ds_read_b64_tr_b16 v[16:17], v2 offset:40960\n"
    "  v_mfma_f32_16x16x32_bf16 v[204:207], v[44:47], v[60:63], v[204:207]\n"
    "  ds_read_b64_tr_b16 v[18:19], v2 offset:51200\n"
    "  v_mfma_f32_16x16x32_bf16 v[208:211], v[48:51], v[60:63], v[208:211]\n"
    "  ds_read_b64_tr_b16 v[20:21], v13 offset:40960\n"
    "  v_mfma_f32_16x16x32_bf16 v[212:215], v[52:55], v[60:63], v[212:215]\n"
    "  ds_read_b64_tr_b16 v[22:23], v13 offset:51200\n"
    "  v_mfma_f32_16x16x32_bf16 v[216:219], v[36:39], v[64:67], v[216:219]\n"
    "  ds_read_b64_tr_b16 v[24:25], v2 offset:43008\n"
    "  v_mfma_f32_16x16x32_bf16 v[220:223], v[40:43], v[64:67], v[220:223]\n"
    "  ds_read_b64_tr_b16 v[26:27], v2 offset:53248\n"
    "  v_mfma_f32_16x16x32_bf16 v[224:227], v[44:47], v[64:67], v[224:227]\n"
    "  ds_read_b64_tr_b16 v[28:29], v13 offset:43008\n"
    "  v_mfma_f32_16x16x32_bf16 v[228:231], v[48:51], v[64:67], v[228:231]\n"
    "  ds_read_b64_tr_b16 v[30:31], v13 offset:53248\n"
    "  v_mfma_f32_16x16x32_bf16 v[232:235], v[52:55], v[64:67], v[232:235]\n"
    "  ds_read_b64_tr_b16 v[32:33], v14 offset:40960\n"
    "  s_waitcnt lgkmcnt(11)\n"
    "  v_mfma_f32_16x16x32_bf16 v[236:239], v[36:39], v[68:71], v[236:239]\n"
    "  ds_read_b64_tr_b16 v[34:35], v14 offset:51200\n"
    "  v_mfma_f32_16x16x32_bf16 v[240:243], v[40:43], v[68:71], v[240:243]\n"
    "  ds_read_b64_tr_b16 v[60:61], v5 offset:16384\n"
    "  v_mfma_f32_16x16x32_bf16 v[244:247], v[44:47], v[68:71], v[244:247]\n"
    "  ds_read_b64_tr_b16 v[62:63], v5 offset:20480\n"
    "  v_mfma_f32_16x16x32_bf16 v[248:251], v[48:51], v[68:71], v[248:251]\n"
    "  ds_read_b64_tr_b16 v[64:65], v6 offset:16384\n"
    "  v_mfma_f32_16x16x32_bf16 v[252:255], v[52:55], v[68:71], v[252:255]\n"
    "  s_waitcnt lgkmcnt(14)\n"
    "  ds_read_b64_tr_b16 v[66:67], v6 offset:20480\n"
    "  s_waitcnt lgkmcnt(12)\n"
    "  v_mfma_f32_16x16x32_bf16 v[96:99], v[16:19], v[56:59], v[96:99]\n"
    "  ds_read_b64_tr_b16 v[68:69], v7 offset:16384\n"
    "  s_waitcnt lgkmcnt(11)\n"
    "  v_mfma_f32_16x16x32_bf16 v[100:103], v[20:23], v[56:59], v[100:103]\n"
    "  ds_read_b64_tr_b16 v[70:71], v7 offset:20480\n"
    "  s_waitcnt lgkmcnt(10)\n"
    "  v_mfma_f32_16x16x32_bf16 v[104:107], v[24:27], v[56:59], v[104:107]\n"
    "  ds_add_u32 v10, v12 offset:0\n"
    "  s_waitcnt lgkmcnt(9)\n"
    "  v_mfma_f32_16x16x32_bf16 v[108:111], v[28:31], v[56:59], v[108:111]\n"
    "  s_waitcnt lgkmcnt(7)\n"
    "  v_mfma_f32_16x16x32_bf16 v[112:115], v[32:35], v[56:59], v[112:115]\n"
    "  s_waitcnt lgkmcnt(5)\n"
    "  v_mfma_f32_16x16x32_bf16 v[116:119], v[16:19], v[60:63], v[116:119]\n"
    "  ds_read_b64_tr_b16 v[56:57], v4 offset:18432\n"
    "  v_mfma_f32_16x16x32_bf16 v[120:123], v[20:23], v[60:63], v[120:123]\n"
    "  ds_read_b64_tr_b16 v[58:59], v4 offset:22528\n"
    "  v_mfma_f32_16x16x32_bf16 v[124:127], v[24:27], v[60:63], v[124:127]\n"
    "  v_mfma_f32_16x16x32_bf16 v[128:131], v[28:31], v[60:63], v[128:131]\n"
    "  v_mfma_f32_16x16x32_bf16 v[132:135], v[32:35], v[60:63], v[132:135]\n"
    "  s_waitcnt lgkmcnt(5)\n"
    "  v_mfma_f32_16x16x32_bf16 v[136:139], v[16:19], v[64:67], v[136:139]\n"
    "  ds_read_b64_tr_b16 v[60:61], v5 offset:18432\n"
    "  v_mfma_f32_16x16x32_bf16 v[140:143], v[20:23], v[64:67], v[140:143]\n"
    "  ds_read_b64_tr_b16 v[62:63], v5 offset:22528\n"
    "  v_mfma_f32_16x16x32_bf16 v[144:147], v[24:27], v[64:67], v[144:147]\n"
    "  v_mfma_f32_16x16x32_bf16 v[148:151], v[28:31], v[64:67], v[148:151]\n"
    "  v_mfma_f32_16x16x32_bf16 v[152:155], v[32:35], v[64:67], v[152:155]\n"
    "  s_waitcnt lgkmcnt(5)\n"
    "  v_mfma_f32_16x16x32_bf16 v[156:159], v[16:19], v[68:71], v[156:159]\n"
    "  ds_read_b64_tr_b16 v[64:65], v6 offset:18432\n"
    "  v_mfma_f32_16x16x32_bf16 v[160:163], v[20:23], v[68:71], v[160:163]\n"
    "  ds_read_b64_tr_b16 v[66:67], v6 offset:22528\n"
    "  v_mfma_f32_16x16x32_bf16 v[164:167], v[24:27], v[68:71], v[164:167]\n"
    "  v_mfma_f32_16x16x32_bf16 v[168:171], v[28:31], v[68:71], v[168:171]\n"
    "  v_mfma_f32_16x16x32_bf16 v[172:175], v[32:35], v[68:71], v[172:175]\n"
    "  ds_read_b32 v11, v15 offset:0\n"
    "  s_waitcnt lgkmcnt(5)\n"
    "  v_mfma_f32_16x16x32_bf16 v[176:179], v[16:19], v[56:59], v[176:179]\n"
    "  ds_read_b64_tr_b16 v[68:69], v7 offset:18432\n"
    "  v_mfma_f32_16x16x32_bf16 v[180:183], v[20:23], v[56:59], v[180:183]\n"
    "  ds_read_b64_tr_b16 v[70:71], v7 offset:22528\n"
    "  v_mfma_f32_16x16x32_bf16 v[184:187], v[24:27], v[56:59], v[184:187]\n"
    "  v_mfma_f32_16x16x32_bf16 v[188:191], v[28:31], v[56:59], v[188:191]\n"
    "  s_waitcnt lgkmcnt(2)\n"
    "  v_cmp_gt_u32_e32 vcc, 0xb0000008, v11\n"
    "  s_cbranch_vccnz .Ledm_sl40\n"
    ".Ledm_bk41:\n"
    "  v_mfma_f32_16x16x32_bf16 v[192:195], v[32:35], v[56:59], v[192:195]\n"
    "  ds_read_b64_tr_b16 v[56:57], v4 offset:24576\n"
    "  v_mfma_f32_16x16x32_bf16 v[196:199], v[16:19], v[60:63], v[196:199]\n"
    "  ds_read_b64_tr_b16 v[58:59], v4 offset:28672\n"
    "  v_mfma_f32_16x16x32_bf16 v[200:203], v[20:23], v[60:63], v[200:203]\n"
    "  ds_read_b64_tr_b16 v[36:37], v2 offset:61440\n"
    "  v_mfma_f32_16x16x32_bf16 v[204:207], v[24:27], v[60:63], v[204:207]\n"
    "  ds_read_b64_tr_b16 v[38:39], v2 offset:0\n"
    "  v_mfma_f32_16x16x32_bf16 v[208:211], v[28:31], v[60:63], v[208:211]\n"
    "  ds_read_b64_tr_b16 v[40:41], v13 offset:61440\n"
    "  v_mfma_f32_16x16x32_bf16 v[212:215], v[32:35], v[60:63], v[212:215]\n"
    "  ds_read_b64_tr_b16 v[42:43], v13 offset:0\n"
    "  v_mfma_f32_16x16x32_bf16 v[216:219], v[16:19], v[64:67], v[216:219]\n"
    "  ds_read_b64_tr_b16 v[44:45], v2 offset:63488\n"
    "  v_mfma_f32_16x16x32_bf16 v[220:223], v[20:23], v[64:67], v[220:223]\n"
    "  ds_read_b64_tr_b16 v[46:47], v2 offset:2048\n"
    "  v_mfma_f32_16x16x32_bf16 v[224:227], v[24:27], v[64:67], v[224:227]\n"
    "  ds_read_b64_tr_b16 v[48:49], v13 offset:63488\n"
    "  v_mfma_f32_16x16x32_bf16 v[228:231], v[28:31], v[64:67], v[228:231]\n"
    "  ds_read_b64_tr_b16 v[50:51], v13 offset:2048\n"
    "  v_mfma_f32_16x16x32_bf16 v[232:235], v[32:35], v[64:67], v[232:235]\n"
    "  ds_read_b64_tr_b16 v[52:53], v14 offset:61440\n"
    "  s_waitcnt lgkmcnt(11)\n"
    "  v_mfma_f32_16x16x32_bf16 v[236:239], v[16:19], v[68:71], v[236:239]\n"
    "  ds_read_b64_tr_b16 v[54:55], v14 offset:0\n"
    "  v_mfma_f32_16x16x32_bf16 v[240:243], v[20:23], v[68:71], v[240:243]\n"
    "  ds_read_b64_tr_b16 v[60:61], v5 offset:24576\n"
    "  v_mfma_f32_16x16x32_bf16 v[244:247], v[24:27], v[68:71], v[244:247]\n"
    "  ds_read_b64_tr_b16 v[62:63], v5 offset:28672\n"
    "  v_mfma_f32_16x16x32_bf16 v[248:251], v[28:31], v[68:71], v[248:251]\n"
    "  ds_read_b64_tr_b16 v[64:65], v6 offset:24576\n"
    "  v_mfma_f32_16x16x32_bf16 v[252:255], v[32:35], v[68:71], v[252:255]\n"
    "  s_waitcnt lgkmcnt(14)\n"
    "  ds_read_b64_tr_b16 v[66:67], v6 offset:28672\n"
    "  s_waitcnt lgkmcnt(12)\n"
    "  v_mfma_f32_16x16x32_bf16 v[96:99], v[36:39], v[56:59], v[96:99]\n"
    "  ds_read_b64_tr_b16 v[68:69], v7 offset:24576\n"
    "  s_waitcnt lgkmcnt(11)\n"
    "  v_mfma_f32_16x16x32_bf16 v[100:103], v[40:43], v[56:59], v[100:103]\n"
    "  ds_read_b64_tr_b16 v[70:71], v7 offset:28672\n"
    "  s_waitcnt lgkmcnt(10)\n"
    "  v_mfma_f32_16x16x32_bf16 v[104:107], v[44:47], v[56:59], v[104:107]\n"
    "  ds_add_u32 v10, v12 offset:0\n"
    "  s_waitcnt lgkmcnt(9)\n"
    "  v_mfma_f32_16x16x32_bf16 v[108:111], v[48:51], v[56:59], v[108:111]\n"
    "  s_waitcnt lgkmcnt(7)\n"
    "  v_mfma_f32_16x16x32_bf16 v[112:115], v[52:55], v[56:59], v[112:115]\n"
    "  s_waitcnt lgkmcnt(5)\n"
    "  v_mfma_f32_16x16x32_bf16 v[116:119], v[36:39], v[60:63], v[116:119]\n"
    "  ds_read_b64_tr_b16 v[56:57], v4 offset:26624\n"
    "  v_mfma_f32_16x16x32_bf16 v[120:123], v[40:43], v[60:63], v[120:123]\n"
    "  ds_read_b64_tr_b16 v[58:59], v4 offset:30720\n"
    "  v_mfma_f32_16x16x32_bf16 v[124:127], v[44:47], v[60:63], v[124:127]\n"
    "  v_mfma_f32_16x16x32_bf16 v[128:131], v[48:51], v[60:63], v[128:131]\n"
    "  v_mfma_f32_16x16x32_bf16 v[132:135], v[52:55], v[60:63], v[132:135]\n"
    "  s_waitcnt lgkmcnt(5)\n"
    "  v_mfma_f32_16x16x32_bf16 v[136:139], v[36:39], v[64:67], v[136:139]\n"
    "  ds_read_b64_tr_b16 v[60:61], v5 offset:26624\n"
    "  v_mfma_f32_16x16x32_bf16 v[140:143], v[40:43], v[64:67], v[140:143]\n"
    "  ds_read_b64_tr_b16 v[62:63], v5 offset:30720\n"
    "  v_mfma_f32_16x16x32_bf16 v[144:147], v[44:47], v[64:67], v[144:147]\n"
    "  v_mfma_f32_16x16x32_bf16 v[148:151], v[48:51], v[64:67], v[148:151]\n"
    "  v_mfma_f32_16x16x32_bf16 v[152:155], v[52:55], v[64:67], v[152:155]\n"
    "  s_waitcnt lgkmcnt(5)\n"
    "  v_mfma_f32_16x16x32_bf16 v[156:159], v[36:39], v[68:71], v[156:159]\n"
    "  ds_read_b64_tr_b16 v[64:65], v6 offset:26624\n"
    "  v_mfma_f32_16x16x32_bf16 v[160:163], v[40:43], v[68:71], v[160:163]\n"
    "  ds_read_b64_tr_b16 v[66:67], v6 offset:30720\n"
    "  v_mfma_f32_16x16x32_bf16 v[164:167], v[44:47], v[68:71], v[164:167]\n"
    "  v_mfma_f32_16x16x32_bf16 v[168:171], v[48:51], v[68:71], v[168:171]\n"
    "  v_mfma_f32_16x16x32_bf16 v[172:175], v[52:55], v[68:71], v[172:175]\n"
    "  ds_read_b32 v11, v15 offset:0\n"
    "  s_waitcnt lgkmcnt(5)\n"
    "  v_mfma_f32_16x16x32_bf16 v[176:179], v[36:39], v[56:59], v[176:179]\n"
    "  ds_read_b64_tr_b16 v[68:69], v7 offset:26624\n"
    "  v_mfma_f32_16x16x32_bf16 v[180:183], v[40:43], v[56:59], v[180:183]\n"
    "  ds_read_b64_tr_b16 v[70:71], v7 offset:30720\n"
    "  v_mfma_f32_16x16x32_bf16 v[184:187], v[44:47], v[56:59], v[184:187]\n"
    "  v_mfma_f32_16x16x32_bf16 v[188:191], v[48:51], v[56:59], v[188:191]\n"
    "  s_waitcnt lgkmcnt(2)\n"
    "  v_cmp_gt_u32_e32 vcc, 0xb000000a, v11\n"
    "  s_cbranch_vccnz .Ledm_sl42\n"
    ".Ledm_bk43:\n"
    "  v_mfma_f32_16x16x32_bf16 v[192:195], v[52:55], v[56:59], v[192:195]\n"
    "  ds_read_b64_tr_b16 v[56:57], v4 offset:32768\n"
    "  v_mfma_f32_16x16x32_bf16 v[196:199], v[36:39], v[60:63], v[196:199]\n"
    "  ds_read_b64_tr_b16 v[58:59], v4 offset:36864\n"
    "  v_mfma_f32_16x16x32_bf16 v[200:203], v[40:43], v[60:63], v[200:203]\n"
    "  ds_read_b64_tr_b16 v[16:17], v2 offset:10240\n"
    "  v_mfma_f32_16x16x32_bf16 v[204:207], v[44:47], v[60:63], v[204:207]\n"
    "  ds_read_b64_tr_b16 v[18:19], v2 offset:20480\n"
    "  v_mfma_f32_16x16x32_bf16 v[208:211], v[48:51], v[60:63], v[208:211]\n"
    "  ds_read_b64_tr_b16 v[20:21], v13 offset:10240\n"
    "  v_mfma_f32_16x16x32_bf16 v[212:215], v[52:55], v[60:63], v[212:215]\n"
    "  ds_read_b64_tr_b16 v[22:23], v13 offset:20480\n"
    "  v_mfma_f32_16x16x32_bf16 v[216:219], v[36:39], v[64:67], v[216:219]\n"
    "  ds_read_b64_tr_b16 v[24:25], v2 offset:12288\n"
    "  v_mfma_f32_16x16x32_bf16 v[220:223], v[40:43], v[64:67], v[220:223]\n"
    "  ds_read_b64_tr_b16 v[26:27], v2 offset:22528\n"
    "  v_mfma_f32_16x16x32_bf16 v[224:227], v[44:47], v[64:67], v[224:227]\n"
    "  ds_read_b64_tr_b16 v[28:29], v13 offset:12288\n"
    "  v_mfma_f32_16x16x32_bf16 v[228:231], v[48:51], v[64:67], v[228:231]\n"
    "  ds_read_b64_tr_b16 v[30:31], v13 offset:22528\n"
    "  v_mfma_f32_16x16x32_bf16 v[232:235], v[52:55], v[64:67], v[232:235]\n"
    "  ds_read_b64_tr_b16 v[32:33], v14 offset:10240\n"
    "  s_waitcnt lgkmcnt(11)\n"
    "  v_mfma_f32_16x16x32_bf16 v[236:239], v[36:39], v[68:71], v[236:239]\n"
    "  ds_read_b64_tr_b16 v[34:35], v14 offset:20480\n"
    "  v_mfma_f32_16x16x32_bf16 v[240:243], v[40:43], v[68:71], v[240:243]\n"
    "  ds_read_b64_tr_b16 v[60:61], v5 offset:32768\n"
    "  v_mfma_f32_16x16x32_bf16 v[244:247], v[44:47], v[68:71], v[244:247]\n"
    "  ds_read_b64_tr_b16 v[62:63], v5 offset:36864\n"
    "  v_mfma_f32_16x16x32_bf16 v[248:251], v[48:51], v[68:71], v[248:251]\n"
    "  ds_read_b64_tr_b16 v[64:65], v6 offset:32768\n"
    "  v_mfma_f32_16x16x32_bf16 v[252:255], v[52:55], v[68:71], v[252:255]\n"
    "  s_waitcnt lgkmcnt(14)\n"
    "  ds_read_b64_tr_b16 v[66:67], v6 offset:36864\n"
    "  s_waitcnt lgkmcnt(12)\n"
    "  v_mfma_f32_16x16x32_bf16 v[96:99], v[16:19], v[56:59], v[96:99]\n"
    "  ds_read_b64_tr_b16 v[68:69], v7 offset:32768\n"
    "  v_lshrrev_b32 v92, 4, v3\n"
    "  s_waitcnt lgkmcnt(11)\n"
    "  v_mfma_f32_16x16x32_bf16 v[100:103], v[20:23], v[56:59], v[100:103]\n"
    "  ds_read_b64_tr_b16 v[70:71], v7 offset:36864\n"
    "  v_lshlrev_b32 v92, 3, v92\n"
    "  s_waitcnt lgkmcnt(10)\n"
    "  v_mfma_f32_16x16x32_bf16 v[104:107], v[24:27], v[56:59], v[104:107]\n"
    "  ds_add_u32 v10, v12 offset:0\n"
    "  s_lshl_b32 s23, s21, 5\n"
    "  s_waitcnt lgkmcnt(9)\n"
    "  v_mfma_f32_16x16x32_bf16 v[108:111], v[28:31], v[56:59], v[108:111]\n"
    "  s_add_u32 s26, s23, 156672\n"
    "  v_add_u32 v93, s26, v92\n"
    "  s_waitcnt lgkmcnt(7)\n"
    "  v_mfma_f32_16x16x32_bf16 v[112:115], v[32:35], v[56:59], v[112:115]\n"
    "  v_add_u32 v92, s23, v93\n"
    "  ds_read_b64 v[72:73], v92 offset:0\n"
    "  s_waitcnt lgkmcnt(6)\n"
    "  v_mfma_f32_16x16x32_bf16 v[116:119], v[16:19], v[60:63], v[116:119]\n"
    "  ds_read_b64_tr_b16 v[56:57], v4 offset:34816\n"
    "  ds_read_b64 v[76:77], v92 offset:32\n"
    "  v_mfma_f32_16x16x32_bf16 v[120:123], v[20:23], v[60:63], v[120:123]\n"
    "  ds_read_b64_tr_b16 v[58:59], v4 offset:38912\n"
    "  ds_read_b64 v[80:81], v92 offset:256\n"
    "  v_mfma_f32_16x16x32_bf16 v[124:127], v[24:27], v[60:63], v[124:127]\n"
    "  ds_read_b64 v[84:85], v92 offset:288\n"
    "  ds_read_b64 v[88:89], v93 offset:512\n"
    "  v_mfma_f32_16x16x32_bf16 v[128:131], v[28:31], v[60:63], v[128:131]\n"
    "  s_lshl_b32 s23, s28, 4\n"
    "  s_lshr_b32 s38, s38, s23\n"
    "  v_mfma_f32_16x16x32_bf16 v[132:135], v[32:35], v[60:63], v[132:135]\n"
    "  s_lshl_b32 s38, s38, 16\n"
    "  v_mul_f32 v92, s38, s38\n"
    "  s_waitcnt lgkmcnt(10)\n"
    "  v_mfma_f32_16x16x32_bf16 v[136:139], v[16:19], v[64:67], v[136:139]\n"
    "  ds_read_b64_tr_b16 v[60:61], v5 offset:34816\n"
    "  v_cvt_pk_bf16_f32 v92, v92, v92\n"
    "  v_mfma_f32_16x16x32_bf16 v[140:143], v[20:23], v[64:67], v[140:143]\n"
    "  ds_read_b64_tr_b16 v[62:63], v5 offset:38912\n"
    "  v_lshlrev_b32 v92, 16, v92\n"
    "  v_mfma_f32_16x16x32_bf16 v[144:147], v[24:27], v[64:67], v[144:147]\n"
    "  v_add_f32 v92, 0x3e800000, v92\n"
    "  v_cvt_pk_bf16_f32 v92, v92, v92\n"
    "  v_mfma_f32_16x16x32_bf16 v[148:151], v[28:31], v[64:67], v[148:151]\n"
    "  v_lshlrev_b32 v92, 16, v92\n"
    "  v_mfma_f32_16x16x32_bf16 v[152:155], v[32:35], v[64:67], v[152:155]\n"
    "  v_sqrt_f32 v93, v92\n"
    "  s_waitcnt lgkmcnt(10)\n"
    "  v_mfma_f32_16x16x32_bf16 v[156:159], v[16:19], v[68:71], v[156:159]\n"
    "  ds_read_b64_tr_b16 v[64:65], v6 offset:34816\n"
    "  v_cvt_pk_bf16_f32 v93, v93, v93\n"
    "  v_mfma_f32_16x16x32_bf16 v[160:163], v[20:23], v[68:71], v[160:163]\n"
    "  ds_read_b64_tr_b16 v[66:67], v6 offset:38912\n"
    "  v_lshlrev_b32 v93, 16, v93\n"
    "  v_mfma_f32_16x16x32_bf16 v[164:167], v[24:27], v[68:71], v[164:167]\n"
    "  v_rcp_f32 v92, v93\n"
    "  v_mfma_f32_16x16x32_bf16 v[168:171], v[28:31], v[68:71], v[168:171]\n"
    "  v_mul_f32 v94, 0.5, s38\n"
    "  v_cvt_pk_bf16_f32 v94, v94, v94\n"
    "  v_mfma_f32_16x16x32_bf16 v[172:175], v[32:35], v[68:71], v[172:175]\n"
    "  ds_read_b32 v11, v15 offset:0\n"
    "  v_lshlrev_b32 v94, 16, v94\n"
    "  s_waitcnt lgkmcnt(8)\n"
    "  v_mfma_f32_16x16x32_bf16 v[176:179], v[16:19], v[56:59], v[176:179]\n"
    "  ds_read_b64_tr_b16 v[68:69], v7 offset:34816\n"
    "  v_mul_f32 v94, v94, v92\n"
    "  v_mfma_f32_16x16x32_bf16 v[180:183], v[20:23], v[56:59], v[180:183]\n"
    "  ds_read_b64_tr_b16 v[70:71], v7 offset:38912\n"
    "  v_cvt_pk_bf16_f32 v94, v94, v94\n"
    "  v_mfma_f32_16x16x32_bf16 v[184:187], v[24:27], v[56:59], v[184:187]\n"
    "  v_lshlrev_b32 v94, 16, v94\n"
    "  v_cvt_pk_bf16_f32 v92, v92, v92\n"
    "  v_mfma_f32_16x16x32_bf16 v[188:191], v[28:31], v[56:59], v[188:191]\n"
    "  s_waitcnt lgkmcnt(2)\n"
    "  v_cmp_gt_u32_e32 vcc, 0xb000000c, v11\n"
    "  s_cbranch_vccnz .Ledm_sl44\n"
    ".Ledm_bk45:\n"
    "  v_mfma_f32_16x16x32_bf16 v[192:195], v[32:35], v[56:59], v[192:195]\n"
    "  ds_read_b64_tr_b16 v[56:57], v4 offset:40960\n"
    "  v_lshlrev_b32 v92, 16, v92\n"
    "  v_mfma_f32_16x16x32_bf16 v[196:199], v[16:19], v[60:63], v[196:199]\n"
    "  ds_read_b64_tr_b16 v[58:59], v4 offset:45056\n"
    "  v_mul_f32 v92, 0xbfb8aa3b, v92\n"
    "  v_mfma_f32_16x16x32_bf16 v[200:203], v[20:23], v[60:63], v[200:203]\n"
    "  ds_read_b64_tr_b16 v[36:37], v2 offset:30720\n"
    "  v_rcp_f32 v94, v94\n"
    "  v_mfma_f32_16x16x32_bf16 v[204:207], v[24:27], v[60:63], v[204:207]\n"
    "  ds_read_b64_tr_b16 v[38:39], v2 offset:40960\n"
    "  v_log_f32 v95, s38\n"
    "  v_mfma_f32_16x16x32_bf16 v[208:211], v[28:31], v[60:63], v[208:211]\n"
    "  ds_read_b64_tr_b16 v[40:41], v13 offset:30720\n"
    "  v_readfirstlane_b32 s36, v92\n"
    "  v_mfma_f32_16x16x32_bf16 v[212:215], v[32:35], v[60:63], v[212:215]\n"
    "  ds_read_b64_tr_b16 v[42:43], v13 offset:40960\n"
    "  v_mul_f32 v94, 0xbfb8aa3b, v94\n"
    "  v_mfma_f32_16x16x32_bf16 v[216:219], v[16:19], v[64:67], v[216:219]\n"
    "  ds_read_b64_tr_b16 v[44:45], v2 offset:32768\n"
    "  v_mul_f32 v95, 0x3f317218, v95\n"
    "  v_mfma_f32_16x16x32_bf16 v[220:223], v[20:23], v[64:67], v[220:223]\n"
    "  ds_read_b64_tr_b16 v[46:47], v2 offset:43008\n"
    "  v_max_f32 v94, 0xff61b1e6, v94\n"
    "  v_mfma_f32_16x16x32_bf16 v[224:227], v[24:27], v[64:67], v[224:227]\n"
    "  ds_read_b64_tr_b16 v[48:49], v13 offset:32768\n"
    "  v_cvt_pk_bf16_f32 v95, v95, v95\n"
    "  v_mfma_f32_16x16x32_bf16 v[228:231], v[28:31], v[64:67], v[228:231]\n"
    "  ds_read_b64_tr_b16 v[50:51], v13 offset:43008\n"
    "  v_lshlrev_b32 v95, 16, v95\n"
    "  v_mfma_f32_16x16x32_bf16 v[232:235], v[32:35], v[64:67], v[232:235]\n"
    "  ds_read_b64_tr_b16 v[52:53], v14 offset:30720\n"
    "  v_readfirstlane_b32 s54, v94\n"
    "  s_waitcnt lgkmcnt(11)\n"
    "  v_mfma_f32_16x16x32_bf16 v[236:239], v[16:19], v[68:71], v[236:239]\n"
    "  ds_read_b64_tr_b16 v[54:55], v14 offset:40960\n"
    "  v_mul_f32 v95, 0x3e800000, v95\n"
    "  v_mfma_f32_16x16x32_bf16 v[240:243], v[20:23], v[68:71], v[240:243]\n"
    "  ds_read_b64_tr_b16 v[60:61], v5 offset:40960\n"
    "  v_cvt_pk_bf16_f32 v95, v95, v95\n"
    "  v_mfma_f32_16x16x32_bf16 v[244:247], v[24:27], v[68:71], v[244:247]\n"
    "  ds_read_b64_tr_b16 v[62:63], v5 offset:45056\n"
    "  v_lshlrev_b32 v95, 16, v95\n"
    "  v_mfma_f32_16x16x32_bf16 v[248:251], v[28:31], v[68:71], v[248:251]\n"
    "  ds_read_b64_tr_b16 v[64:65], v6 offset:40960\n"
    "  v_and_b32 v75, 0xffff0000, v73\n"
    "  v_mfma_f32_16x16x32_bf16 v[252:255], v[32:35], v[68:71], v[252:255]\n"
    "  s_waitcnt lgkmcnt(14)\n"
    "  ds_read_b64_tr_b16 v[66:67], v6 offset:45056\n"
    "  v_lshlrev_b32 v74, 16, v73\n"
    "  s_waitcnt lgkmcnt(12)\n"
    "  v_mfma_f32_16x16x32_bf16 v[96:99], v[36:39], v[56:59], v[96:99]\n"
    "  ds_read_b64_tr_b16 v[68:69], v7 offset:40960\n"
    "  v_and_b32 v73, 0xffff0000, v72\n"
    "  s_waitcnt lgkmcnt(11)\n"
    "  v_mfma_f32_16x16x32_bf16 v[100:103], v[40:43], v[56:59], v[100:103]\n"
    "  ds_read_b64_tr_b16 v[70:71], v7 offset:45056\n"
    "  v_lshlrev_b32 v72, 16, v72\n"
    "  s_waitcnt lgkmcnt(10)\n"
    "  v_mfma_f32_16x16x32_bf16 v[104:107], v[44:47], v[56:59], v[104:107]\n"
    "  ds_add_u32 v10, v12 offset:0\n"
    "  v_mul_f32 v72, v95, v72\n"
    "  s_waitcnt lgkmcnt(9)\n"
    "  v_mfma_f32_16x16x32_bf16 v[108:111], v[48:51], v[56:59], v[108:111]\n"
    "  v_mul_f32 v73, v95, v73\n"
    "  v_mul_f32 v74, v95, v74\n"
    "  s_waitcnt lgkmcnt(7)\n"
    "  v_mfma_f32_16x16x32_bf16 v[112:115], v[52:55], v[56:59], v[112:115]\n"
    "  v_mul_f32 v75, v95, v75\n"
    "  v_cvt_pk_bf16_f32 v72, v72, v73\n"
    "  s_waitcnt lgkmcnt(5)\n"
    "  v_mfma_f32_16x16x32_bf16 v[116:119], v[36:39], v[60:63], v[116:119]\n"
    "  ds_read_b64_tr_b16 v[56:57], v4 offset:43008\n"
    "  v_cvt_pk_bf16_f32 v74, v74, v75\n"
    "  v_mfma_f32_16x16x32_bf16 v[120:123], v[40:43], v[60:63], v[120:123]\n"
    "  ds_read_b64_tr_b16 v[58:59], v4 offset:47104\n"
    "  v_and_b32 v73, 0xffff0000, v72\n"
    "  v_mfma_f32_16x16x32_bf16 v[124:127], v[44:47], v[60:63], v[124:127]\n"
    "  v_and_b32 v75, 0xffff0000, v74\n"
    "  v_lshlrev_b32 v72, 16, v72\n"
    "  v_mfma_f32_16x16x32_bf16 v[128:131], v[48:51], v[60:63], v[128:131]\n"
    "  v_lshlrev_b32 v74, 16, v74\n"
    "  v_mul_f32 v72, 0xbfb8aa3b, v72\n"
    "  v_mfma_f32_16x16x32_bf16 v[132:135], v[52:55], v[60:63], v[132:135]\n"
    "  v_mul_f32 v73, 0xbfb8aa3b, v73\n"
    "  v_mul_f32 v74, 0xbfb8aa3b, v74\n"
    "  s_waitcnt lgkmcnt(5)\n"
    "  v_mfma_f32_16x16x32_bf16 v[136:139], v[36:39], v[64:67], v[136:139]\n"
    "  ds_read_b64_tr_b16 v[60:61], v5 offset:43008\n"
    "  v_mul_f32 v75, 0xbfb8aa3b, v75\n"
    "  v_mfma_f32_16x16x32_bf16 v[140:143], v[40:43], v[64:67], v[140:143]\n"
    "  ds_read_b64_tr_b16 v[62:63], v5 offset:47104\n"
    "  v_and_b32 v79, 0xffff0000, v77\n"
    "  v_mfma_f32_16x16x32_bf16 v[144:147], v[44:47], v[64:67], v[144:147]\n"
    "  v_lshlrev_b32 v78, 16, v77\n"
    "  v_and_b32 v77, 0xffff0000, v76\n"
    "  v_mfma_f32_16x16x32_bf16 v[148:151], v[48:51], v[64:67], v[148:151]\n"
    "  v_lshlrev_b32 v76, 16, v76\n"
    "  v_mul_f32 v76, v95, v76\n"
    "  v_mfma_f32_16x16x32_bf16 v[152:155], v[52:55], v[64:67], v[152:155]\n"
    "  v_mul_f32 v77, v95, v77\n"
    "  v_mul_f32 v78, v95, v78\n"
    "  s_waitcnt lgkmcnt(5)\n"
    "  v_mfma_f32_16x16x32_bf16 v[156:159], v[36:39], v[68:71], v[156:159]\n"
    "  ds_read_b64_tr_b16 v[64:65], v6 offset:43008\n"
    "  v_mul_f32 v79, v95, v79\n"
    "  v_mfma_f32_16x16x32_bf16 v[160:163], v[40:43], v[68:71], v[160:163]\n"
    "  ds_read_b64_tr_b16 v[66:67], v6 offset:47104\n"
    "  v_cvt_pk_bf16_f32 v76, v76, v77\n"
    "  v_mfma_f32_16x16x32_bf16 v[164:167], v[44:47], v[68:71], v[164:167]\n"
    "  v_cvt_pk_bf16_f32 v78, v78, v79\n"
    "  v_and_b32 v77, 0xffff0000, v76\n"
    "  v_mfma_f32_16x16x32_bf16 v[168:171], v[48:51], v[68:71], v[168:171]\n"
    "  v_and_b32 v79, 0xffff0000, v78\n"
    "  v_lshlrev_b32 v76, 16, v76\n"
    "  v_mfma_f32_16x16x32_bf16 v[172:175], v[52:55], v[68:71], v[172:175]\n"
    "  ds_read_b32 v11, v15 offset:0\n"
    "  v_lshlrev_b32 v78, 16, v78\n"
    "  s_waitcnt lgkmcnt(5)\n"
    "  v_mfma_f32_16x16x32_bf16 v[176:179], v[36:39], v[56:59], v[176:179]\n"
    "  ds_read_b64_tr_b16 v[68:69], v7 offset:43008\n"
    "  v_mul_f32 v76, 0xbfb8aa3b, v76\n"
    "  v_mfma_f32_16x16x32_bf16 v[180:183], v[40:43], v[56:59], v[180:183]\n"
    "  ds_read_b64_tr_b16 v[70:71], v7 offset:47104\n"
    "  v_mul_f32 v77, 0xbfb8aa3b, v77\n"
    "  v_mfma_f32_16x16x32_bf16 v[184:187], v[44:47], v[56:59], v[184:187]\n"
    "  v_mul_f32 v78, 0xbfb8aa3b, v78\n"
    "  v_mul_f32 v79, 0xbfb8aa3b, v79\n"
    "  v_mfma_f32_16x16x32_bf16 v[188:191], v[48:51], v[56:59], v[188:191]\n"
    "  s_waitcnt lgkmcnt(2)\n"
    "  v_cmp_gt_u32_e32 vcc, 0xb000000e, v11\n"
    "  s_cbranch_vccnz .Ledm_sl46\n"
    ".Ledm_bk47:\n"
    "  v_mfma_f32_16x16x32_bf16 v[192:195], v[52:55], v[56:59], v[192:195]\n"
    "  ds_read_b64_tr_b16 v[56:57], v4 offset:49152\n"
    "  v_and_b32 v83, 0xffff0000, v81\n"
    "  v_mfma_f32_16x16x32_bf16 v[196:199], v[36:39], v[60:63], v[196:199]\n"
    "  ds_read_b64_tr_b16 v[58:59], v4 offset:53248\n"
    "  v_lshlrev_b32 v82, 16, v81\n"
    "  v_mfma_f32_16x16x32_bf16 v[200:203], v[40:43], v[60:63], v[200:203]\n"
    "  ds_read_b64_tr_b16 v[16:17], v2 offset:51200\n"
    "  v_and_b32 v81, 0xffff0000, v80\n"
    "  v_mfma_f32_16x16x32_bf16 v[204:207], v[44:47], v[60:63], v[204:207]\n"
    "  ds_read_b64_tr_b16 v[18:19], v2 offset:61440\n"
    "  v_lshlrev_b32 v80, 16, v80\n"
    "  v_mfma_f32_16x16x32_bf16 v[208:211], v[48:51], v[60:63], v[208:211]\n"
    "  ds_read_b64_tr_b16 v[20:21], v13 offset:51200\n"
    "  v_mul_f32 v80, v95, v80\n"
    "  v_mfma_f32_16x16x32_bf16 v[212:215], v[52:55], v[60:63], v[212:215]\n"
    "  ds_read_b64_tr_b16 v[22:23], v13 offset:61440\n"
    "  v_mul_f32 v81, v95, v81\n"
    "  v_mfma_f32_16x16x32_bf16 v[216:219], v[36:39], v[64:67], v[216:219]\n"
    "  ds_read_b64_tr_b16 v[24:25], v2 offset:53248\n"
    "  v_mul_f32 v82, v95, v82\n"
    "  v_mfma_f32_16x16x32_bf16 v[220:223], v[40:43], v[64:67], v[220:223]\n"
    "  ds_read_b64_tr_b16 v[26:27], v2 offset:63488\n"
    "  v_mul_f32 v83, v95, v83\n"
    "  v_mfma_f32_16x16x32_bf16 v[224:227], v[44:47], v[64:67], v[224:227]\n"
    "  ds_read_b64_tr_b16 v[28:29], v13 offset:53248\n"
    "  v_cvt_pk_bf16_f32 v80, v80, v81\n"
    "  v_mfma_f32_16x16x32_bf16 v[228:231], v[48:51], v[64:67], v[228:231]\n"
    "  ds_read_b64_tr_b16 v[30:31], v13 offset:63488\n"
    "  v_cvt_pk_bf16_f32 v82, v82, v83\n"
    "  v_mfma_f32_16x16x32_bf16 v[232:235], v[52:55], v[64:67], v[232:235]\n"
    "  ds_read_b64_tr_b16 v[32:33], v14 offset:51200\n"
    "  v_and_b32 v81, 0xffff0000, v80\n"
    "  s_waitcnt lgkmcnt(11)\n"
    "  v_mfma_f32_16x16x32_bf16 v[236:239], v[36:39], v[68:71], v[236:239]\n"
    "  ds_read_b64_tr_b16 v[34:35], v14 offset:61440\n"
    "  v_and_b32 v83, 0xffff0000, v82\n"
    "  v_mfma_f32_16x16x32_bf16 v[240:243], v[40:43], v[68:71], v[240:243]\n"
    "  ds_read_b64_tr_b16 v[60:61], v5 offset:49152\n"
    "  v_lshlrev_b32 v80, 16, v80\n"
    "  v_mfma_f32_16x16x32_bf16 v[244:247], v[44:47], v[68:71], v[244:247]\n"
    "  ds_read_b64_tr_b16 v[62:63], v5 offset:53248\n"
    "  v_lshlrev_b32 v82, 16, v82\n"
    "  v_mfma_f32_16x16x32_bf16 v[248:251], v[48:51], v[68:71], v[248:251]\n"
    "  ds_read_b64_tr_b16 v[64:65], v6 offset:49152\n"
    "  v_mul_f32 v80, 0xbfb8aa3b, v80\n"
    "  v_mfma_f32_16x16x32_bf16 v[252:255], v[52:55], v[68:71], v[252:255]\n"
    "  s_waitcnt lgkmcnt(14)\n"
    "  ds_read_b64_tr_b16 v[66:67], v6 offset:53248\n"
    "  v_mul_f32 v81, 0xbfb8aa3b, v81\n"
    "  s_waitcnt lgkmcnt(12)\n"
    "  v_mfma_f32_16x16x32_bf16 v[96:99], v[16:19], v[56:59], v[96:99]\n"
    "  ds_read_b64_tr_b16 v[68:69], v7 offset:49152\n"
    "  v_mul_f32 v82, 0xbfb8aa3b, v82\n"
    "  s_waitcnt lgkmcnt(11)\n"
    "  v_mfma_f32_16x16x32_bf16 v[100:103], v[20:23], v[56:59], v[100:103]\n"
    "  ds_read_b64_tr_b16 v[70:71], v7 offset:53248\n"
    "  v_mul_f32 v83, 0xbfb8aa3b, v83\n"
    "  s_waitcnt lgkmcnt(10)\n"
    "  v_mfma_f32_16x16x32_bf16 v[104:107], v[24:27], v[56:59], v[104:107]\n"
    "  ds_add_u32 v10, v12 offset:0\n"
    "  v_and_b32 v87, 0xffff0000, v85\n"
    "  s_waitcnt lgkmcnt(9)\n"
    "  v_mfma_f32_16x16x32_bf16 v[108:111], v[28:31], v[56:59], v[108:111]\n"
    "  v_lshlrev_b32 v86, 16, v85\n"
    "  v_and_b32 v85, 0xffff0000, v84\n"
    "  s_waitcnt lgkmcnt(7)\n"
    "  v_mfma_f32_16x16x32_bf16 v[112:115], v[32:35], v[56:59], v[112:115]\n"
    "  v_lshlrev_b32 v84, 16, v84\n"
    "  v_mul_f32 v84, v95, v84\n"
    "  s_waitcnt lgkmcnt(5)\n"
    "  v_mfma_f32_16x16x32_bf16 v[116:119], v[16:19], v[60:63], v[116:119]\n"
    "  ds_read_b64_tr_b16 v[56:57], v4 offset:51200\n"
    "  v_mul_f32 v85, v95, v85\n"
    "  v_mfma_f32_16x16x32_bf16 v[120:123], v[20:23], v[60:63], v[120:123]\n"
    "  ds_read_b64_tr_b16 v[58:59], v4 offset:55296\n"
    "  v_mul_f32 v86, v95, v86\n"
    "  v_mfma_f32_16x16x32_bf16 v[124:127], v[24:27], v[60:63], v[124:127]\n"
    "  v_mul_f32 v87, v95, v87\n"
    "  v_cvt_pk_bf16_f32 v84, v84, v85\n"
    "  v_mfma_f32_16x16x32_bf16 v[128:131], v[28:31], v[60:63], v[128:131]\n"
    "  v_cvt_pk_bf16_f32 v86, v86, v87\n"
    "  v_and_b32 v85, 0xffff0000, v84\n"
    "  v_mfma_f32_16x16x32_bf16 v[132:135], v[32:35], v[60:63], v[132:135]\n"
    "  v_and_b32 v87, 0xffff0000, v86\n"
    "  v_lshlrev_b32 v84, 16, v84\n"
    "  s_waitcnt lgkmcnt(5)\n"
    "  v_mfma_f32_16x16x32_bf16 v[136:139], v[16:19], v[64:67], v[136:139]\n"
    "  ds_read_b64_tr_b16 v[60:61], v5 offset:51200\n"
    "  v_lshlrev_b32 v86, 16, v86\n"
    "  v_mfma_f32_16x16x32_bf16 v[140:143], v[20:23], v[64:67], v[140:143]\n"
    "  ds_read_b64_tr_b16 v[62:63], v5 offset:55296\n"
    "  v_mul_f32 v84, 0xbfb8aa3b, v84\n"
    "  v_mfma_f32_16x16x32_bf16 v[144:147], v[24:27], v[64:67], v[144:147]\n"
    "  v_mul_f32 v85, 0xbfb8aa3b, v85\n"
    "  v_mul_f32 v86, 0xbfb8aa3b, v86\n"
    "  v_mfma_f32_16x16x32_bf16 v[148:151], v[28:31], v[64:67], v[148:151]\n"
    "  v_mul_f32 v87, 0xbfb8aa3b, v87\n"
    "  v_and_b32 v91, 0xffff0000, v89\n"
    "  v_mfma_f32_16x16x32_bf16 v[152:155], v[32:35], v[64:67], v[152:155]\n"
    "  v_lshlrev_b32 v90, 16, v89\n"
    "  v_and_b32 v89, 0xffff0000, v88\n"
    "  s_waitcnt lgkmcnt(5)\n"
    "  v_mfma_f32_16x16x32_bf16 v[156:159], v[16:19], v[68:71], v[156:159]\n"
    "  ds_read_b64_tr_b16 v[64:65], v6 offset:51200\n"
    "  v_lshlrev_b32 v88, 16, v88\n"
    "  v_mfma_f32_16x16x32_bf16 v[160:163], v[20:23], v[68:71], v[160:163]\n"
    "  ds_read_b64_tr_b16 v[66:67], v6 offset:55296\n"
    "  v_mul_f32 v88, v95, v88\n"
    "  v_mfma_f32_16x16x32_bf16 v[164:167], v[24:27], v[68:71], v[164:167]\n"
    "  v_mul_f32 v89, v95, v89\n"
    "  v_mul_f32 v90, v95, v90\n"
    "  v_mfma_f32_16x16x32_bf16 v[168:171], v[28:31], v[68:71], v[168:171]\n"
    "  v_mul_f32 v91, v95, v91\n"
    "  v_cvt_pk_bf16_f32 v88, v88, v89\n"
    "  v_mfma_f32_16x16x32_bf16 v[172:175], v[32:35], v[68:71], v[172:175]\n"
    "  ds_read_b32 v11, v15 offset:0\n"
    "  v_cvt_pk_bf16_f32 v90, v90, v91\n"
    "  s_waitcnt lgkmcnt(5)\n"
    "  v_mfma_f32_16x16x32_bf16 v[176:179], v[16:19], v[56:59], v[176:179]\n"
    "  ds_read_b64_tr_b16 v[68:69], v7 offset:51200\n"
    "  v_and_b32 v89, 0xffff0000, v88\n"
    "  v_mfma_f32_16x16x32_bf16 v[180:183], v[20:23], v[56:59], v[180:183]\n"
    "  ds_read_b64_tr_b16 v[70:71], v7 offset:55296\n"
    "  v_and_b32 v91, 0xffff0000, v90\n"
    "  v_mfma_f32_16x16x32_bf16 v[184:187], v[24:27], v[56:59], v[184:187]\n"
    "  v_lshlrev_b32 v88, 16, v88\n"
    "  v_lshlrev_b32 v90, 16, v90\n"
    "  v_mfma_f32_16x16x32_bf16 v[188:191], v[28:31], v[56:59], v[188:191]\n"
    "  s_waitcnt lgkmcnt(2)\n"
    "  v_cmp_gt_u32_e32 vcc, 0xb0000010, v11\n"
    "  s_cbranch_vccnz .Ledm_sl48\n"
    ".Ledm_bk49:\n"
    "  v_mfma_f32_16x16x32_bf16 v[192:195], v[32:35], v[56:59], v[192:195]\n"
    "  ds_read_b64_tr_b16 v[56:57], v4 offset:57344\n"
    "  v_mul_f32 v88, 0xbfb8aa3b, v88\n"
    "  v_mfma_f32_16x16x32_bf16 v[196:199], v[16:19], v[60:63], v[196:199]\n"
    "  ds_read_b64_tr_b16 v[58:59], v4 offset:61440\n"
    "  v_mul_f32 v89, 0xbfb8aa3b, v89\n"
    "  v_mfma_f32_16x16x32_bf16 v[200:203], v[20:23], v[60:63], v[200:203]\n"
    "  ds_read_b64_tr_b16 v[36:37], v2 offset:0\n"
    "  v_mul_f32 v90, 0xbfb8aa3b, v90\n"
    "  v_mfma_f32_16x16x32_bf16 v[204:207], v[24:27], v[60:63], v[204:207]\n"
    "  ds_read_b64_tr_b16 v[38:39], v2 offset:10240\n"
    "  v_mul_f32 v91, 0xbfb8aa3b, v91\n"
    "  v_mfma_f32_16x16x32_bf16 v[208:211], v[28:31], v[60:63], v[208:211]\n"
    "  ds_read_b64_tr_b16 v[40:41], v13 offset:0\n"
    "  v_mfma_f32_16x16x32_bf16 v[212:215], v[32:35], v[60:63], v[212:215]\n"
    "  ds_read_b64_tr_b16 v[42:43], v13 offset:10240\n"
    "  v_mfma_f32_16x16x32_bf16 v[216:219], v[16:19], v[64:67], v[216:219]\n"
    "  ds_read_b64_tr_b16 v[44:45], v2 offset:2048\n"
    "  v_mfma_f32_16x16x32_bf16 v[220:223], v[20:23], v[64:67], v[220:223]\n"
    "  ds_read_b64_tr_b16 v[46:47], v2 offset:12288\n"
    "  v_mfma_f32_16x16x32_bf16 v[224:227], v[24:27], v[64:67], v[224:227]\n"
    "  ds_read_b64_tr_b16 v[48:49], v13 offset:2048\n"
    "  v_mfma_f32_16x16x32_bf16 v[228:231], v[28:31], v[64:67], v[228:231]\n"
    "  ds_read_b64_tr_b16 v[50:51], v13 offset:12288\n"
    "  v_mfma_f32_16x16x32_bf16 v[232:235], v[32:35], v[64:67], v[232:235]\n"
    "  ds_read_b64_tr_b16 v[52:53], v14 offset:0\n"
    "  s_waitcnt lgkmcnt(11)\n"
    "  v_mfma_f32_16x16x32_bf16 v[236:239], v[16:19], v[68:71], v[236:239]\n"
    "  ds_read_b64_tr_b16 v[54:55], v14 offset:10240\n"
    "  v_mfma_f32_16x16x32_bf16 v[240:243], v[20:23], v[68:71], v[240:243]\n"
    "  ds_read_b64_tr_b16 v[60:61], v5 offset:57344\n"
    "  v_mfma_f32_16x16x32_bf16 v[244:247], v[24:27], v[68:71], v[244:247]\n"
    "  ds_read_b64_tr_b16 v[62:63], v5 offset:61440\n"
    "  v_mfma_f32_16x16x32_bf16 v[248:251], v[28:31], v[68:71], v[248:251]\n"
    "  ds_read_b64_tr_b16 v[64:65], v6 offset:57344\n"
    "  v_mfma_f32_16x16x32_bf16 v[252:255], v[32:35], v[68:71], v[252:255]\n"
    "  s_waitcnt lgkmcnt(14)\n"
    "  ds_read_b64_tr_b16 v[66:67], v6 offset:61440\n"
    "  s_waitcnt lgkmcnt(12)\n"
    "  v_mfma_f32_16x16x32_bf16 v[96:99], v[36:39], v[56:59], v[96:99]\n"
    "  ds_read_b64_tr_b16 v[68:69], v7 offset:57344\n"
    "  s_waitcnt lgkmcnt(11)\n"
    "  v_mfma_f32_16x16x32_bf16 v[100:103], v[40:43], v[56:59], v[100:103]\n"
    "  ds_read_b64_tr_b16 v[70:71], v7 offset:61440\n"
    "  s_waitcnt lgkmcnt(10)\n"
    "  v_mfma_f32_16x16x32_bf16 v[104:107], v[44:47], v[56:59], v[104:107]\n"
    "  ds_add_u32 v10, v12 offset:0\n"
    "  s_waitcnt lgkmcnt(9)\n"
    "  v_mfma_f32_16x16x32_bf16 v[108:111], v[48:51], v[56:59], v[108:111]\n"
    "  s_waitcnt lgkmcnt(7)\n"
    "  v_mfma_f32_16x16x32_bf16 v[112:115], v[52:55], v[56:59], v[112:115]\n"
    "  s_waitcnt lgkmcnt(5)\n"
    "  v_mfma_f32_16x16x32_bf16 v[116:119], v[36:39], v[60:63], v[116:119]\n"
    "  ds_read_b64_tr_b16 v[56:57], v4 offset:59392\n"
    "  v_mfma_f32_16x16x32_bf16 v[120:123], v[40:43], v[60:63], v[120:123]\n"
    "  ds_read_b64_tr_b16 v[58:59], v4 offset:63488\n"
    "  v_mfma_f32_16x16x32_bf16 v[124:127], v[44:47], v[60:63], v[124:127]\n"
    "  v_mfma_f32_16x16x32_bf16 v[128:131], v[48:51], v[60:63], v[128:131]\n"
    "  v_mfma_f32_16x16x32_bf16 v[132:135], v[52:55], v[60:63], v[132:135]\n"
    "  s_waitcnt lgkmcnt(5)\n"
    "  v_mfma_f32_16x16x32_bf16 v[136:139], v[36:39], v[64:67], v[136:139]\n"
    "  ds_read_b64_tr_b16 v[60:61], v5 offset:59392\n"
    "  v_mfma_f32_16x16x32_bf16 v[140:143], v[40:43], v[64:67], v[140:143]\n"
    "  ds_read_b64_tr_b16 v[62:63], v5 offset:63488\n"
    "  v_mfma_f32_16x16x32_bf16 v[144:147], v[44:47], v[64:67], v[144:147]\n"
    "  v_mfma_f32_16x16x32_bf16 v[148:151], v[48:51], v[64:67], v[148:151]\n"
    "  v_mfma_f32_16x16x32_bf16 v[152:155], v[52:55], v[64:67], v[152:155]\n"
    "  s_waitcnt lgkmcnt(5)\n"
    "  v_mfma_f32_16x16x32_bf16 v[156:159], v[36:39], v[68:71], v[156:159]\n"
    "  ds_read_b64_tr_b16 v[64:65], v6 offset:59392\n"
    "  v_mfma_f32_16x16x32_bf16 v[160:163], v[40:43], v[68:71], v[160:163]\n"
    "  ds_read_b64_tr_b16 v[66:67], v6 offset:63488\n"
    "  v_mfma_f32_16x16x32_bf16 v[164:167], v[44:47], v[68:71], v[164:167]\n"
    "  v_mfma_f32_16x16x32_bf16 v[168:171], v[48:51], v[68:71], v[168:171]\n"
    "  v_mfma_f32_16x16x32_bf16 v[172:175], v[52:55], v[68:71], v[172:175]\n"
    "  ds_read_b32 v11, v15 offset:0\n"
    "  s_waitcnt lgkmcnt(5)\n"
    "  v_mfma_f32_16x16x32_bf16 v[176:179], v[36:39], v[56:59], v[176:179]\n"
    "  ds_read_b64_tr_b16 v[68:69], v7 offset:59392\n"
    "  v_mfma_f32_16x16x32_bf16 v[180:183], v[40:43], v[56:59], v[180:183]\n"
    "  ds_read_b64_tr_b16 v[70:71], v7 offset:63488\n"
    "  v_mfma_f32_16x16x32_bf16 v[184:187], v[44:47], v[56:59], v[184:187]\n"
    "  v_mfma_f32_16x16x32_bf16 v[188:191], v[48:51], v[56:59], v[188:191]\n"
    "  s_waitcnt lgkmcnt(2)\n"
    "  v_cmp_gt_u32_e32 vcc, 0xb0000012, v11\n"
    "  s_cbranch_vccnz .Ledm_sl50\n"
    ".Ledm_bk51:\n"
    "  v_mfma_f32_16x16x32_bf16 v[192:195], v[52:55], v[56:59], v[192:195]\n"
    "  v_add_u32 v4, 65536, v4\n"
    "  ds_read_b64_tr_b16 v[56:57], v4 offset:0\n"
    "  v_mfma_f32_16x16x32_bf16 v[196:199], v[36:39], v[60:63], v[196:199]\n"
    "  ds_read_b64_tr_b16 v[58:59], v4 offset:4096\n"
    "  v_mfma_f32_16x16x32_bf16 v[200:203], v[40:43], v[60:63], v[200:203]\n"
    "  ds_read_b64_tr_b16 v[16:17], v2 offset:20480\n"
    "  v_mfma_f32_16x16x32_bf16 v[204:207], v[44:47], v[60:63], v[204:207]\n"
    "  ds_read_b64_tr_b16 v[18:19], v2 offset:30720\n"
    "  v_mfma_f32_16x16x32_bf16 v[208:211], v[48:51], v[60:63], v[208:211]\n"
    "  ds_read_b64_tr_b16 v[20:21], v13 offset:20480\n"
    "  v_mfma_f32_16x16x32_bf16 v[212:215], v[52:55], v[60:63], v[212:215]\n"
    "  ds_read_b64_tr_b16 v[22:23], v13 offset:30720\n"
    "  v_mfma_f32_16x16x32_bf16 v[216:219], v[36:39], v[64:67], v[216:219]\n"
    "  ds_read_b64_tr_b16 v[24:25], v2 offset:22528\n"
    "  v_mfma_f32_16x16x32_bf16 v[220:223], v[40:43], v[64:67], v[220:223]\n"
    "  ds_read_b64_tr_b16 v[26:27], v2 offset:32768\n"
    "  v_mfma_f32_16x16x32_bf16 v[224:227], v[44:47], v[64:67], v[224:227]\n"
    "  ds_read_b64_tr_b16 v[28:29], v13 offset:22528\n"
    "  v_mfma_f32_16x16x32_bf16 v[228:231], v[48:51], v[64:67], v[228:231]\n"
    "  ds_read_b64_tr_b16 v[30:31], v13 offset:32768\n"
    "  v_mfma_f32_16x16x32_bf16 v[232:235], v[52:55], v[64:67], v[232:235]\n"
    "  ds_read_b64_tr_b16 v[32:33], v14 offset:20480\n"
    "  s_waitcnt lgkmcnt(11)\n"
    "  v_mfma_f32_16x16x32_bf16 v[236:239], v[36:39], v[68:71], v[236:239]\n"
    "  ds_read_b64_tr_b16 v[34:35], v14 offset:30720\n"
    "  v_mfma_f32_16x16x32_bf16 v[240:243], v[40:43], v[68:71], v[240:243]\n"
    "  v_add_u32 v5, 65536, v5\n"
    "  ds_read_b64_tr_b16 v[60:61], v5 offset:0\n"
    "  v_mfma_f32_16x16x32_bf16 v[244:247], v[44:47], v[68:71], v[244:247]\n"
    "  ds_read_b64_tr_b16 v[62:63], v5 offset:4096\n"
    "  v_mfma_f32_16x16x32_bf16 v[248:251], v[48:51], v[68:71], v[248:251]\n"
    "  v_add_u32 v6, 65536, v6\n"
    "  ds_read_b64_tr_b16 v[64:65], v6 offset:0\n"
    "  v_mfma_f32_16x16x32_bf16 v[252:255], v[52:55], v[68:71], v[252:255]\n"
    "  s_waitcnt lgkmcnt(14)\n"
    "  ds_read_b64_tr_b16 v[66:67], v6 offset:4096\n"
    "  v_add_u32 v7, 65536, v7\n"
    "  s_waitcnt lgkmcnt(12)\n"
    "  v_mfma_f32_16x16x32_bf16 v[96:99], v[16:19], v[56:59], v[96:99]\n"
    "  ds_read_b64_tr_b16 v[68:69], v7 offset:0\n"
    "  s_waitcnt lgkmcnt(11)\n"
    "  v_mfma_f32_16x16x32_bf16 v[100:103], v[20:23], v[56:59], v[100:103]\n"
    "  ds_read_b64_tr_b16 v[70:71], v7 offset:4096\n"
    "  s_waitcnt lgkmcnt(10)\n"
    "  v_mfma_f32_16x16x32_bf16 v[104:107], v[24:27], v[56:59], v[104:107]\n"
    "  ds_add_u32 v10, v12 offset:0\n"
    "  s_waitcnt lgkmcnt(9)\n"
    "  v_mfma_f32_16x16x32_bf16 v[108:111], v[28:31], v[56:59], v[108:111]\n"
    "  s_waitcnt lgkmcnt(7)\n"
    "  v_mfma_f32_16x16x32_bf16 v[112:115], v[32:35], v[56:59], v[112:115]\n"
    "  s_waitcnt lgkmcnt(5)\n"
    "  v_mfma_f32_16x16x32_bf16 v[116:119], v[16:19], v[60:63], v[116:119]\n"
    "  ds_read_b64_tr_b16 v[56:57], v4 offset:2048\n"
    "  v_mfma_f32_16x16x32_bf16 v[120:123], v[20:23], v[60:63], v[120:123]\n"
    "  ds_read_b64_tr_b16 v[58:59], v4 offset:6144\n"
    "  v_mfma_f32_16x16x32_bf16 v[124:127], v[24:27], v[60:63], v[124:127]\n"
    "  v_mfma_f32_16x16x32_bf16 v[128:131], v[28:31], v[60:63], v[128:131]\n"
    "  v_mfma_f32_16x16x32_bf16 v[132:135], v[32:35], v[60:63], v[132:135]\n"
    "  s_waitcnt lgkmcnt(5)\n"
    "  v_mfma_f32_16x16x32_bf16 v[136:139], v[16:19], v[64:67], v[136:139]\n"
    "  ds_read_b64_tr_b16 v[60:61], v5 offset:2048\n"
    "  v_mfma_f32_16x16x32_bf16 v[140:143], v[20:23], v[64:67], v[140:143]\n"
    "  ds_read_b64_tr_b16 v[62:63], v5 offset:6144\n"
    "  v_mfma_f32_16x16x32_bf16 v[144:147], v[24:27], v[64:67], v[144:147]\n"
    "  v_mfma_f32_16x16x32_bf16 v[148:151], v[28:31], v[64:67], v[148:151]\n"
    "  v_mfma_f32_16x16x32_bf16 v[152:155], v[32:35], v[64:67], v[152:155]\n"
    "  s_waitcnt lgkmcnt(5)\n"
    "  v_mfma_f32_16x16x32_bf16 v[156:159], v[16:19], v[68:71], v[156:159]\n"
    "  ds_read_b64_tr_b16 v[64:65], v6 offset:2048\n"
    "  v_mfma_f32_16x16x32_bf16 v[160:163], v[20:23], v[68:71], v[160:163]\n"
    "  ds_read_b64_tr_b16 v[66:67], v6 offset:6144\n"
    "  v_mfma_f32_16x16x32_bf16 v[164:167], v[24:27], v[68:71], v[164:167]\n"
    "  v_mfma_f32_16x16x32_bf16 v[168:171], v[28:31], v[68:71], v[168:171]\n"
    "  v_mfma_f32_16x16x32_bf16 v[172:175], v[32:35], v[68:71], v[172:175]\n"
    "  ds_read_b32 v11, v15 offset:0\n"
    "  s_waitcnt lgkmcnt(5)\n"
    "  v_mfma_f32_16x16x32_bf16 v[176:179], v[16:19], v[56:59], v[176:179]\n"
    "  ds_read_b64_tr_b16 v[68:69], v7 offset:2048\n"
    "  v_mfma_f32_16x16x32_bf16 v[180:183], v[20:23], v[56:59], v[180:183]\n"
    "  ds_read_b64_tr_b16 v[70:71], v7 offset:6144\n"
    "  v_mfma_f32_16x16x32_bf16 v[184:187], v[24:27], v[56:59], v[184:187]\n"
    "  v_mfma_f32_16x16x32_bf16 v[188:191], v[28:31], v[56:59], v[188:191]\n"
    "  s_waitcnt lgkmcnt(2)\n"
    "  v_cmp_gt_u32_e32 vcc, 0xb0000014, v11\n"
    "  s_cbranch_vccnz .Ledm_sl52\n"
    ".Ledm_bk53:\n"
    "  v_mfma_f32_16x16x32_bf16 v[192:195], v[32:35], v[56:59], v[192:195]\n"
    "  ds_read_b64_tr_b16 v[56:57], v4 offset:8192\n"
    "  v_mfma_f32_16x16x32_bf16 v[196:199], v[16:19], v[60:63], v[196:199]\n"
    "  ds_read_b64_tr_b16 v[58:59], v4 offset:12288\n"
    "  v_mfma_f32_16x16x32_bf16 v[200:203], v[20:23], v[60:63], v[200:203]\n"
    "  ds_read_b64_tr_b16 v[36:37], v2 offset:40960\n"
    "  v_mfma_f32_16x16x32_bf16 v[204:207], v[24:27], v[60:63], v[204:207]\n"
    "  ds_read_b64_tr_b16 v[38:39], v2 offset:51200\n"
    "  v_mfma_f32_16x16x32_bf16 v[208:211], v[28:31], v[60:63], v[208:211]\n"
    "  ds_read_b64_tr_b16 v[40:41], v13 offset:40960\n"
    "  v_mfma_f32_16x16x32_bf16 v[212:215], v[32:35], v[60:63], v[212:215]\n"
    "  ds_read_b64_tr_b16 v[42:43], v13 offset:51200\n"
    "  v_mfma_f32_16x16x32_bf16 v[216:219], v[16:19], v[64:67], v[216:219]\n"
    "  ds_read_b64_tr_b16 v[44:45], v2 offset:43008\n"
    "  v_mfma_f32_16x16x32_bf16 v[220:223], v[20:23], v[64:67], v[220:223]\n"
    "  ds_read_b64_tr_b16 v[46:47], v2 offset:53248\n"
    "  v_mfma_f32_16x16x32_bf16 v[224:227], v[24:27], v[64:67], v[224:227]\n"
    "  ds_read_b64_tr_b16 v[48:49], v13 offset:43008\n"
    "  v_mfma_f32_16x16x32_bf16 v[228:231], v[28:31], v[64:67], v[228:231]\n"
    "  ds_read_b64_tr_b16 v[50:51], v13 offset:53248\n"
    "  v_mfma_f32_16x16x32_bf16 v[232:235], v[32:35], v[64:67], v[232:235]\n"
    "  ds_read_b64_tr_b16 v[52:53], v14 offset:40960\n"
    "  s_waitcnt lgkmcnt(11)\n"
    "  v_mfma_f32_16x16x32_bf16 v[236:239], v[16:19], v[68:71], v[236:239]\n"
    "  ds_read_b64_tr_b16 v[54:55], v14 offset:51200\n"
    "  v_mfma_f32_16x16x32_bf16 v[240:243], v[20:23], v[68:71], v[240:243]\n"
    "  ds_read_b64_tr_b16 v[60:61], v5 offset:8192\n"
    "  v_mfma_f32_16x16x32_bf16 v[244:247], v[24:27], v[68:71], v[244:247]\n"
    "  ds_read_b64_tr_b16 v[62:63], v5 offset:12288\n"
    "  v_mfma_f32_16x16x32_bf16 v[248:251], v[28:31], v[68:71], v[248:251]\n"
    "  ds_read_b64_tr_b16 v[64:65], v6 offset:8192\n"
    "  v_mfma_f32_16x16x32_bf16 v[252:255], v[32:35], v[68:71], v[252:255]\n"
    "  s_waitcnt lgkmcnt(14)\n"
    "  ds_read_b64_tr_b16 v[66:67], v6 offset:12288\n"
    "  s_waitcnt lgkmcnt(12)\n"
    "  v_mfma_f32_16x16x32_bf16 v[96:99], v[36:39], v[56:59], v[96:99]\n"
    "  ds_read_b64_tr_b16 v[68:69], v7 offset:8192\n"
    "  s_waitcnt lgkmcnt(11)\n"
    "  v_mfma_f32_16x16x32_bf16 v[100:103], v[40:43], v[56:59], v[100:103]\n"
    "  ds_read_b64_tr_b16 v[70:71], v7 offset:12288\n"
    "  s_waitcnt lgkmcnt(10)\n"
    "  v_mfma_f32_16x16x32_bf16 v[104:107], v[44:47], v[56:59], v[104:107]\n"
    "  ds_add_u32 v10, v12 offset:0\n"
    "  s_waitcnt lgkmcnt(9)\n"
    "  v_mfma_f32_16x16x32_bf16 v[108:111], v[48:51], v[56:59], v[108:111]\n"
    "  v_and_b32 v2, 3, v3\n"
    "  v_mul_u32_u24 v2, 0x104, v2\n"
    "  s_waitcnt lgkmcnt(7)\n"
    "  v_mfma_f32_16x16x32_bf16 v[112:115], v[52:55], v[56:59], v[112:115]\n"
    "  v_add_u32 v2, 153600, v2\n"
    "  v_mov_b32 v15, 1\n"
    "  s_waitcnt lgkmcnt(5)\n"
    "  v_mfma_f32_16x16x32_bf16 v[116:119], v[36:39], v[60:63], v[116:119]\n"
    "  ds_read_b64_tr_b16 v[56:57], v4 offset:10240\n"
    "  v_mfma_f32_16x16x32_bf16 v[120:123], v[40:43], v[60:63], v[120:123]\n"
    "  ds_read_b64_tr_b16 v[58:59], v4 offset:14336\n"
    "  v_mfma_f32_16x16x32_bf16 v[124:127], v[44:47], v[60:63], v[124:127]\n"
    "  v_mfma_f32_16x16x32_bf16 v[128:131], v[48:51], v[60:63], v[128:131]\n"
    "  v_fma_f32 v96, v96, s36, v72\n"
    "  v_fma_f32 v97, v97, s36, v73\n"
    "  v_mfma_f32_16x16x32_bf16 v[132:135], v[52:55], v[60:63], v[132:135]\n"
    "  v_fma_f32 v98, v98, s36, v74\n"
    "  v_fma_f32 v99, v99, s36, v75\n"
    "  s_waitcnt lgkmcnt(5)\n"
    "  v_mfma_f32_16x16x32_bf16 v[136:139], v[36:39], v[64:67], v[136:139]\n"
    "  ds_read_b64_tr_b16 v[60:61], v5 offset:10240\n"
    "  v_fma_f32 v100, v100, s36, v76\n"
    "  v_fma_f32 v101, v101, s36, v77\n"
    "  v_mfma_f32_16x16x32_bf16 v[140:143], v[40:43], v[64:67], v[140:143]\n"
    "  ds_read_b64_tr_b16 v[62:63], v5 offset:14336\n"
    "  v_fma_f32 v102, v102, s36, v78\n"
    "  v_fma_f32 v103, v103, s36, v79\n"
    "  v_mfma_f32_16x16x32_bf16 v[144:147], v[44:47], v[64:67], v[144:147]\n"
    "  v_exp_f32 v26, v96\n"
    "  v_mfma_f32_16x16x32_bf16 v[148:151], v[48:51], v[64:67], v[148:151]\n"
    "  v_exp_f32 v27, v97\n"
    "  v_mfma_f32_16x16x32_bf16 v[152:155], v[52:55], v[64:67], v[152:155]\n"
    "  v_exp_f32 v28, v98\n"
    "  s_waitcnt lgkmcnt(5)\n"
    "  v_mfma_f32_16x16x32_bf16 v[156:159], v[36:39], v[68:71], v[156:159]\n"
    "  ds_read_b64_tr_b16 v[64:65], v6 offset:10240\n"
    "  v_exp_f32 v29, v99\n"
    "  v_mfma_f32_16x16x32_bf16 v[160:163], v[40:43], v[68:71], v[160:163]\n"
    "  ds_read_b64_tr_b16 v[66:67], v6 offset:14336\n"
    "  v_exp_f32 v30, v100\n"
    "  v_mfma_f32_16x16x32_bf16 v[164:167], v[44:47], v[68:71], v[164:167]\n"
    "  v_exp_f32 v31, v101\n"
    "  v_mfma_f32_16x16x32_bf16 v[168:171], v[48:51], v[68:71], v[168:171]\n"
    "  v_exp_f32 v32, v102\n"
    "  v_mfma_f32_16x16x32_bf16 v[172:175], v[52:55], v[68:71], v[172:175]\n"
    "  v_exp_f32 v33, v103\n"
    "  s_waitcnt lgkmcnt(4)\n"
    "  v_mfma_f32_16x16x32_bf16 v[176:179], v[36:39], v[56:59], v[176:179]\n"
    "  ds_read_b64_tr_b16 v[68:69], v7 offset:10240\n"
    "  v_fma_f32 v26, v26, s54, s54\n"
    "  v_fma_f32 v27, v27, s54, s54\n"
    "  v_mfma_f32_16x16x32_bf16 v[180:183], v[40:43], v[56:59], v[180:183]\n"
    "  ds_read_b64_tr_b16 v[70:71], v7 offset:14336\n"
    "  v_fma_f32 v28, v28, s54, s54\n"
    "  v_fma_f32 v29, v29, s54, s54\n"
    "  v_mfma_f32_16x16x32_bf16 v[184:187], v[44:47], v[56:59], v[184:187]\n"
    "  v_fma_f32 v30, v30, s54, s54\n"
    "  v_fma_f32 v31, v31, s54, s54\n"
    "  v_mfma_f32_16x16x32_bf16 v[188:191], v[48:51], v[56:59], v[188:191]\n"
    "  v_fma_f32 v32, v32, s54, s54\n"
    "  v_fma_f32 v33, v33, s54, s54\n"
    "  v_mfma_f32_16x16x32_bf16 v[192:195], v[52:55], v[56:59], v[192:195]\n"
    "  v_rcp_f32 v26, v26\n"
    "  s_waitcnt lgkmcnt(4)\n"
    "  v_mfma_f32_16x16x32_bf16 v[196:199], v[36:39], v[60:63], v[196:199]\n"
    "  v_rcp_f32 v27, v27\n"
    "  v_mfma_f32_16x16x32_bf16 v[200:203], v[40:43], v[60:63], v[200:203]\n"
    "  v_rcp_f32 v28, v28\n"
    "  v_mfma_f32_16x16x32_bf16 v[204:207], v[44:47], v[60:63], v[204:207]\n"
    "  v_rcp_f32 v29, v29\n"
    "  v_mfma_f32_16x16x32_bf16 v[208:211], v[48:51], v[60:63], v[208:211]\n"
    "  v_rcp_f32 v30, v30\n"
    "  v_mfma_f32_16x16x32_bf16 v[212:215], v[52:55], v[60:63], v[212:215]\n"
    "  v_rcp_f32 v31, v31\n"
    "  s_waitcnt lgkmcnt(2)\n"
    "  v_mfma_f32_16x16x32_bf16 v[216:219], v[36:39], v[64:67], v[216:219]\n"
    "  v_rcp_f32 v32, v32\n"
    "  v_mfma_f32_16x16x32_bf16 v[220:223], v[40:43], v[64:67], v[220:223]\n"
    "  v_rcp_f32 v33, v33\n"
    "  v_mfma_f32_16x16x32_bf16 v[224:227], v[44:47], v[64:67], v[224:227]\n"
    "  v_mul_f32 v96, v96, v26\n"
    "  v_mul_f32 v97, v97, v27\n"
    "  v_mfma_f32_16x16x32_bf16 v[228:231], v[48:51], v[64:67], v[228:231]\n"
    "  v_mul_f32 v98, v98, v28\n"
    "  v_mul_f32 v99, v99, v29\n"
    "  v_mfma_f32_16x16x32_bf16 v[232:235], v[52:55], v[64:67], v[232:235]\n"
    "  v_mul_f32 v100, v100, v30\n"
    "  v_mul_f32 v101, v101, v31\n"
    "  s_waitcnt lgkmcnt(0)\n"
    "  v_mfma_f32_16x16x32_bf16 v[236:239], v[36:39], v[68:71], v[236:239]\n"
    "  v_mul_f32 v102, v102, v32\n"
    "  v_mul_f32 v103, v103, v33\n"
    "  v_mfma_f32_16x16x32_bf16 v[240:243], v[40:43], v[68:71], v[240:243]\n"
    "  v_cvt_pk_bf16_f32 v96, v96, v97\n"
    "  v_cvt_pk_bf16_f32 v97, v98, v99\n"
    "  v_mfma_f32_16x16x32_bf16 v[244:247], v[44:47], v[68:71], v[244:247]\n"
    "  v_cvt_pk_bf16_f32 v98, v100, v101\n"
    "  v_cvt_pk_bf16_f32 v99, v102, v103\n"
    "  v_mfma_f32_16x16x32_bf16 v[248:251], v[48:51], v[68:71], v[248:251]\n"
    "  v_fma_f32 v104, v104, s36, v80\n"
    "  v_fma_f32 v105, v105, s36, v81\n"
    "  v_mfma_f32_16x16x32_bf16 v[252:255], v[52:55], v[68:71], v[252:255]\n"
    "  v_fma_f32 v106, v106, s36, v82\n"
    "  v_fma_f32 v107, v107, s36, v83\n"
    "  s_setprio 0\n"
    "  s_mov_b32 s35, 2\n"
    "  s_mov_b32 s44, 0xeeeeeeee\n"
    "  s_mov_b32 s45, 0xeeeeeeee\n"
    "  v_fma_f32 v108, v108, s36, v84\n"
    "  v_fma_f32 v109, v109, s36, v85\n"
    "  v_fma_f32 v110, v110, s36, v86\n"
    "  v_fma_f32 v111, v111, s36, v87\n"
    "  v_exp_f32 v26, v104\n"
    "  v_exp_f32 v27, v105\n"
    "  v_exp_f32 v28, v106\n"
    "  v_exp_f32 v29, v107\n"
    "  v_exp_f32 v30, v108\n"
    "  v_exp_f32 v31, v109\n"
    "  v_exp_f32 v32, v110\n"
    "  v_exp_f32 v33, v111\n"
    "  v_fma_f32 v26, v26, s54, s54\n"
    "  v_fma_f32 v27, v27, s54, s54\n"
    "  v_fma_f32 v28, v28, s54, s54\n"
    "  v_fma_f32 v29, v29, s54, s54\n"
    "  v_fma_f32 v30, v30, s54, s54\n"
    "  v_fma_f32 v31, v31, s54, s54\n"
    "  v_fma_f32 v32, v32, s54, s54\n"
    "  v_fma_f32 v33, v33, s54, s54\n"
    "  v_rcp_f32 v26, v26\n"
    "  v_rcp_f32 v27, v27\n"
    "  v_rcp_f32 v28, v28\n"
    "  v_rcp_f32 v29, v29\n"
    "  v_rcp_f32 v30, v30\n"
    "  v_rcp_f32 v31, v31\n"
    "  v_rcp_f32 v32, v32\n"
    "  v_rcp_f32 v33, v33\n"
    "  v_mul_f32 v104, v104, v26\n"
    "  v_mul_f32 v105, v105, v27\n"
    "  v_mul_f32 v106, v106, v28\n"
    "  v_mul_f32 v107, v107, v29\n"
    "  v_mul_f32 v108, v108, v30\n"
    "  v_mul_f32 v109, v109, v31\n"
    "  v_mul_f32 v110, v110, v32\n"
    "  v_mul_f32 v111, v111, v33\n"
    "  v_cvt_pk_bf16_f32 v100, v104, v105\n"
    "  v_cvt_pk_bf16_f32 v101, v106, v107\n"
    "  v_cvt_pk_bf16_f32 v102, v108, v109\n"
    "  v_cvt_pk_bf16_f32 v103, v110, v111\n"
    "  v_fma_f32 v112, v112, s36, v88\n"
    "  v_fma_f32 v113, v113, s36, v89\n"
    "  ds_read_b32 v11, v2 offset:0\n"
    "  v_fma_f32 v114, v114, s36, v90\n"
    "  v_fma_f32 v115, v115, s36, v91\n"
    "  v_exp_f32 v26, v112\n"
    "  v_exp_f32 v27, v113\n"
    "  v_exp_f32 v28, v114\n"
    "  v_exp_f32 v29, v115\n"
    "  v_fma_f32 v26, v26, s54, s54\n"
    "  v_fma_f32 v27, v27, s54, s54\n"
    "  v_fma_f32 v28, v28, s54, s54\n"
    "  v_fma_f32 v29, v29, s54, s54\n"
    "  v_rcp_f32 v26, v26\n"
    "  v_rcp_f32 v27, v27\n"
    "  v_rcp_f32 v28, v28\n"
    "  v_rcp_f32 v29, v29\n"
    "  v_mul_f32 v112, v112, v26\n"
    "  v_mul_f32 v113, v113, v27\n"
    "  v_mul_f32 v114, v114, v28\n"
    "  v_mul_f32 v115, v115, v29\n"
    "  v_cvt_pk_bf16_f32 v104, v112, v113\n"
    "  v_cvt_pk_bf16_f32 v105, v114, v115\n"
    "  s_waitcnt lgkmcnt(0)\n"
    "  v_cmp_gt_u32_e32 vcc, 20, v11\n"
    "  s_cbranch_vccnz .Ledm_sl54\n"
    ".Ledm_bk55:\n"
    "  ds_write_b128 v8, v[96:99] offset:40960\n"
    "  ds_write_b128 v8, v[100:103] offset:41984\n"
    "  ds_write_b64 v9, v[104:105] offset:40960\n"
    "  ds_add_u32 v10, v15 offset:0\n"
    "  v_fma_f32 v116, v116, s36, v72\n"
    "  v_fma_f32 v117, v117, s36, v73\n"
    "  v_fma_f32 v118, v118, s36, v74\n"
    "  v_fma_f32 v119, v119, s36, v75\n"
    "  v_fma_f32 v120, v120, s36, v76\n"
    "  v_fma_f32 v121, v121, s36, v77\n"
    "  v_fma_f32 v122, v122, s36, v78\n"
    "  v_fma_f32 v123, v123, s36, v79\n"
    "  v_exp_f32 v16, v116\n"
    "  v_exp_f32 v17, v117\n"
    "  v_exp_f32 v18, v118\n"
    "  v_exp_f32 v19, v119\n"
    "  v_exp_f32 v20, v120\n"
    "  v_exp_f32 v21, v121\n"
    "  v_exp_f32 v22, v122\n"
    "  v_exp_f32 v23, v123\n"
    "  v_fma_f32 v16, v16, s54, s54\n"
    "  v_fma_f32 v17, v17, s54, s54\n"
    "  v_fma_f32 v18, v18, s54, s54\n"
    "  v_fma_f32 v19, v19, s54, s54\n"
    "  v_fma_f32 v20, v20, s54, s54\n"
    "  v_fma_f32 v21, v21, s54, s54\n"
    "  v_fma_f32 v22, v22, s54, s54\n"
    "  v_fma_f32 v23, v23, s54, s54\n"
    "  v_rcp_f32 v16, v16\n"
    "  v_rcp_f32 v17, v17\n"
    "  v_rcp_f32 v18, v18\n"
    "  v_rcp_f32 v19, v19\n"
    "  v_rcp_f32 v20, v20\n"
    "  v_rcp_f32 v21, v21\n"
    "  v_rcp_f32 v22, v22\n"
    "  v_rcp_f32 v23, v23\n"
    "  v_mul_f32 v116, v116, v16\n"
    "  v_mul_f32 v117, v117, v17\n"
    "  v_mul_f32 v118, v118, v18\n"
    "  v_mul_f32 v119, v119, v19\n"
    "  v_mul_f32 v120, v120, v20\n"
    "  v_mul_f32 v121, v121, v21\n"
    "  v_mul_f32 v122, v122, v22\n"
    "  v_mul_f32 v123, v123, v23\n"
    "  v_cvt_pk_bf16_f32 v116, v116, v117\n"
    "  v_cvt_pk_bf16_f32 v117, v118, v119\n"
    "  v_cvt_pk_bf16_f32 v118, v120, v121\n"
    "  v_cvt_pk_bf16_f32 v119, v122, v123\n"
    "  v_fma_f32 v124, v124, s36, v80\n"
    "  v_fma_f32 v125, v125, s36, v81\n"
    "  v_fma_f32 v126, v126, s36, v82\n"
    "  v_fma_f32 v127, v127, s36, v83\n"
    "  v_fma_f32 v128, v128, s36, v84\n"
    "  v_fma_f32 v129, v129, s36, v85\n"
    "  v_fma_f32 v130, v130, s36, v86\n"
    "  v_fma_f32 v131, v131, s36, v87\n"
    "  v_exp_f32 v16, v124\n"
    "  v_exp_f32 v17, v125\n"
    "  v_exp_f32 v18, v126\n"
    "  v_exp_f32 v19, v127\n"
    "  v_exp_f32 v20, v128\n"
    "  v_exp_f32 v21, v129\n"
    "  v_exp_f32 v22, v130\n"
    "  v_exp_f32 v23, v131\n"
    "  v_fma_f32 v16, v16, s54, s54\n"
    "  v_fma_f32 v17, v17, s54, s54\n"
    "  v_fma_f32 v18, v18, s54, s54\n"
    "  v_fma_f32 v19, v19, s54, s54\n"
    "  v_fma_f32 v20, v20, s54, s54\n"
    "  v_fma_f32 v21, v21, s54, s54\n"
    "  v_fma_f32 v22, v22, s54, s54\n"
    "  v_fma_f32 v23, v23, s54, s54\n"
    "  v_rcp_f32 v16, v16\n"
    "  v_rcp_f32 v17, v17\n"
    "  v_rcp_f32 v18, v18\n"
    "  v_rcp_f32 v19, v19\n"
    "  v_rcp_f32 v20, v20\n"
    "  v_rcp_f32 v21, v21\n"
    "  v_rcp_f32 v22, v22\n"
    "  v_rcp_f32 v23, v23\n"
    "  v_mul_f32 v124, v124, v16\n"
    "  v_mul_f32 v125, v125, v17\n"
    "  v_mul_f32 v126, v126, v18\n"
    "  v_mul_f32 v127, v127, v19\n"
    "  v_mul_f32 v128, v128, v20\n"
    "  v_mul_f32 v129, v129, v21\n"
    "  v_mul_f32 v130, v130, v22\n"
    "  v_mul_f32 v131, v131, v23\n"
    "  v_cvt_pk_bf16_f32 v120, v124, v125\n"
    "  v_cvt_pk_bf16_f32 v121, v126, v127\n"
    "  v_cvt_pk_bf16_f32 v122, v128, v129\n"
    "  v_cvt_pk_bf16_f32 v123, v130, v131\n"
    "  v_fma_f32 v132, v132, s36, v88\n"
    "  v_fma_f32 v133, v133, s36, v89\n"
    "  v_fma_f32 v134, v134, s36, v90\n"
    "  v_fma_f32 v135, v135, s36, v91\n"
    "  v_exp_f32 v16, v132\n"
    "  v_exp_f32 v17, v133\n"
    "  v_exp_f32 v18, v134\n"
    "  v_exp_f32 v19, v135\n"
    "  v_fma_f32 v16, v16, s54, s54\n"
    "  v_fma_f32 v17, v17, s54, s54\n"
    "  v_fma_f32 v18, v18, s54, s54\n"
    "  v_fma_f32 v19, v19, s54, s54\n"
    "  v_rcp_f32 v16, v16\n"
    "  v_rcp_f32 v17, v17\n"
    "  v_rcp_f32 v18, v18\n"
    "  v_rcp_f32 v19, v19\n"
    "  v_mul_f32 v132, v132, v16\n"
    "  v_mul_f32 v133, v133, v17\n"
    "  v_mul_f32 v134, v134, v18\n"
    "  v_mul_f32 v135, v135, v19\n"
    "  v_cvt_pk_bf16_f32 v124, v132, v133\n"
    "  v_cvt_pk_bf16_f32 v125, v134, v135\n"
    "  ds_write_b128 v8, v[116:119] offset:51200\n"
    "  ds_write_b128 v8, v[120:123] offset:52224\n"
    "  ds_write_b64 v9, v[124:125] offset:51200\n"
    "  ds_add_u32 v10, v15 offset:0\n"
    "  v_fma_f32 v136, v136, s36, v72\n"
    "  v_fma_f32 v137, v137, s36, v73\n"
    "  v_fma_f32 v138, v138, s36, v74\n"
    "  v_fma_f32 v139, v139, s36, v75\n"
    "  v_fma_f32 v140, v140, s36, v76\n"
    "  v_fma_f32 v141, v141, s36, v77\n"
    "  v_fma_f32 v142, v142, s36, v78\n"
    "  v_fma_f32 v143, v143, s36, v79\n"
    "  v_exp_f32 v16, v136\n"
    "  v_exp_f32 v17, v137\n"
    "  v_exp_f32 v18, v138\n"
    "  v_exp_f32 v19, v139\n"
    "  v_exp_f32 v20, v140\n"
    "  v_exp_f32 v21, v141\n"
    "  v_exp_f32 v22, v142\n"
    "  v_exp_f32 v23, v143\n"
    "  v_fma_f32 v16, v16, s54, s54\n"
    "  v_fma_f32 v17, v17, s54, s54\n"
    "  v_fma_f32 v18, v18, s54, s54\n"
    "  v_fma_f32 v19, v19, s54, s54\n"
    "  v_fma_f32 v20, v20, s54, s54\n"
    "  v_fma_f32 v21, v21, s54, s54\n"
    "  v_fma_f32 v22, v22, s54, s54\n"
    "  v_fma_f32 v23, v23, s54, s54\n"
    "  v_rcp_f32 v16, v16\n"
    "  v_rcp_f32 v17, v17\n"
    "  v_rcp_f32 v18, v18\n"
    "  v_rcp_f32 v19, v19\n"
    "  v_rcp_f32 v20, v20\n"
    "  v_rcp_f32 v21, v21\n"
    "  v_rcp_f32 v22, v22\n"
    "  v_rcp_f32 v23, v23\n"
    "  v_mul_f32 v136, v136, v16\n"
    "  v_mul_f32 v137, v137, v17\n"
    "  v_mul_f32 v138, v138, v18\n"
    "  v_mul_f32 v139, v139, v19\n"
    "  v_mul_f32 v140, v140, v20\n"
    "  v_mul_f32 v141, v141, v21\n"
    "  v_mul_f32 v142, v142, v22\n"
    "  v_mul_f32 v143, v143, v23\n"
    "  v_cvt_pk_bf16_f32 v136, v136, v137\n"
    "  v_cvt_pk_bf16_f32 v137, v138, v139\n"
    "  v_cvt_pk_bf16_f32 v138, v140, v141\n"
    "  v_cvt_pk_bf16_f32 v139, v142, v143\n"
    "  v_fma_f32 v144, v144, s36, v80\n"
    "  v_fma_f32 v145, v145, s36, v81\n"
    "  v_fma_f32 v146, v146, s36, v82\n"
    "  v_fma_f32 v147, v147, s36, v83\n"
    "  v_fma_f32 v148, v148, s36, v84\n"
    "  v_fma_f32 v149, v149, s36, v85\n"
    "  v_fma_f32 v150, v150, s36, v86\n"
    "  v_fma_f32 v151, v151, s36, v87\n"
    "  v_exp_f32 v16, v144\n"
    "  v_exp_f32 v17, v145\n"
    "  v_exp_f32 v18, v146\n"
    "  v_exp_f32 v19, v147\n"
    "  v_exp_f32 v20, v148\n"
    "  v_exp_f32 v21, v149\n"
    "  v_exp_f32 v22, v150\n"
    "  v_exp_f32 v23, v151\n"
    "  v_fma_f32 v16, v16, s54, s54\n"
    "  v_fma_f32 v17, v17, s54, s54\n"
    "  v_fma_f32 v18, v18, s54, s54\n"
    "  v_fma_f32 v19, v19, s54, s54\n"
    "  v_fma_f32 v20, v20, s54, s54\n"
    "  v_fma_f32 v21, v21, s54, s54\n"
    "  v_fma_f32 v22, v22, s54, s54\n"
    "  v_fma_f32 v23, v23, s54, s54\n"
    "  v_rcp_f32 v16, v16\n"
    "  v_rcp_f32 v17, v17\n"
    "  v_rcp_f32 v18, v18\n"
    "  v_rcp_f32 v19, v19\n"
    "  v_rcp_f32 v20, v20\n"
    "  v_rcp_f32 v21, v21\n"
    "  v_rcp_f32 v22, v22\n"
    "  v_rcp_f32 v23, v23\n"
    "  v_mul_f32 v144, v144, v16\n"
    "  v_mul_f32 v145, v145, v17\n"
    "  v_mul_f32 v146, v146, v18\n"
    "  v_mul_f32 v147, v147, v19\n"
    "  v_mul_f32 v148, v148, v20\n"
    "  v_mul_f32 v149, v149, v21\n"
    "  v_mul_f32 v150, v150, v22\n"
    "  v_mul_f32 v151, v151, v23\n"
    "  v_cvt_pk_bf16_f32 v140, v144, v145\n"
    "  v_cvt_pk_bf16_f32 v141, v146, v147\n"
    "  v_cvt_pk_bf16_f32 v142, v148, v149\n"
    "  v_cvt_pk_bf16_f32 v143, v150, v151\n"
    "  v_fma_f32 v152, v152, s36, v88\n"
    "  v_fma_f32 v153, v153, s36, v89\n"
    "  ds_read_b32 v11, v2 offset:1024\n"
    "  v_fma_f32 v154, v154, s36, v90\n"
    "  v_fma_f32 v155, v155, s36, v91\n"
    "  v_exp_f32 v16, v152\n"
    "  v_exp_f32 v17, v153\n"
    "  v_exp_f32 v18, v154\n"
    "  v_exp_f32 v19, v155\n"
    "  v_fma_f32 v16, v16, s54, s54\n"
    "  v_fma_f32 v17, v17, s54, s54\n"
    "  v_fma_f32 v18, v18, s54, s54\n"
    "  v_fma_f32 v19, v19, s54, s54\n"
    "  v_rcp_f32 v16, v16\n"
    "  v_rcp_f32 v17, v17\n"
    "  v_rcp_f32 v18, v18\n"
    "  v_rcp_f32 v19, v19\n"
    "  v_mul_f32 v152, v152, v16\n"
    "  v_mul_f32 v153, v153, v17\n"
    "  v_mul_f32 v154, v154, v18\n"
    "  v_mul_f32 v155, v155, v19\n"
    "  v_cvt_pk_bf16_f32 v144, v152, v153\n"
    "  v_cvt_pk_bf16_f32 v145, v154, v155\n"
    "  s_waitcnt lgkmcnt(0)\n"
    "  v_cmp_gt_u32_e32 vcc, 2, v11\n"
    "  s_branch .Ledm_f56\n"
    ".Ledm_b57:\n"
    "  v_fma_f32 v156, v156, s36, v72\n"
    "  v_fma_f32 v157, v157, s36, v73\n"
    "  v_fma_f32 v158, v158, s36, v74\n"
    "  v_fma_f32 v159, v159, s36, v75\n"
    "  v_fma_f32 v160, v160, s36, v76\n"
    "  v_fma_f32 v161, v161, s36, v77\n"
    "  v_fma_f32 v162, v162, s36, v78\n"
    "  v_fma_f32 v163, v163, s36, v79\n"
    "  v_exp_f32 v16, v156\n"
    "  v_exp_f32 v17, v157\n"
    "  v_exp_f32 v18, v158\n"
    "  v_exp_f32 v19, v159\n"
    "  v_exp_f32 v20, v160\n"
    "  v_exp_f32 v21, v161\n"
    "  v_exp_f32 v22, v162\n"
    "  v_exp_f32 v23, v163\n"
    "  v_fma_f32 v16, v16, s54, s54\n"
    "  v_fma_f32 v17, v17, s54, s54\n"
    "  v_fma_f32 v18, v18, s54, s54\n"
    "  v_fma_f32 v19, v19, s54, s54\n"
    "  v_fma_f32 v20, v20, s54, s54\n"
    "  v_fma_f32 v21, v21, s54, s54\n"
    "  v_fma_f32 v22, v22, s54, s54\n"
    "  v_fma_f32 v23, v23, s54, s54\n"
    "  ds_read_b32 v11, v2 offset:1024\n"
    "  v_rcp_f32 v16, v16\n"
    "  v_rcp_f32 v17, v17\n"
    "  v_rcp_f32 v18, v18\n"
    "  v_rcp_f32 v19, v19\n"
    "  v_rcp_f32 v20, v20\n"
    "  v_rcp_f32 v21, v21\n"
    "  v_rcp_f32 v22, v22\n"
    "  v_rcp_f32 v23, v23\n"
    "  v_mul_f32 v156, v156, v16\n"
    "  v_mul_f32 v157, v157, v17\n"
    "  v_mul_f32 v158, v158, v18\n"
    "  v_mul_f32 v159, v159, v19\n"
    "  s_cmp_eq_u32 s35, 3\n"
    "  s_cbranch_scc1 .Ledm_b60\n"
    "  s_waitcnt lgkmcnt(0)\n"
    "  v_cmp_gt_u32_e32 vcc, 2, v11\n"
    "  s_branch .Ledm_f59\n"
    ".Ledm_b60:\n"
    "  v_mul_f32 v160, v160, v20\n"
    "  v_mul_f32 v161, v161, v21\n"
    "  v_mul_f32 v162, v162, v22\n"
    "  v_mul_f32 v163, v163, v23\n"
    "  v_cvt_pk_bf16_f32 v156, v156, v157\n"
    "  v_cvt_pk_bf16_f32 v157, v158, v159\n"
    "  v_cvt_pk_bf16_f32 v158, v160, v161\n"
    "  v_cvt_pk_bf16_f32 v159, v162, v163\n"
    "  v_fma_f32 v164, v164, s36, v80\n"
    "  v_fma_f32 v165, v165, s36, v81\n"
    "  v_fma_f32 v166, v166, s36, v82\n"
    "  v_fma_f32 v167, v167, s36, v83\n"
    "  v_fma_f32 v168, v168, s36, v84\n"
    "  v_fma_f32 v169, v169, s36, v85\n"
    "  v_fma_f32 v170, v170, s36, v86\n"
    "  v_fma_f32 v171, v171, s36, v87\n"
    "  v_exp_f32 v16, v164\n"
    "  v_exp_f32 v17, v165\n"
    "  v_exp_f32 v18, v166\n"
    "  v_exp_f32 v19, v167\n"
    "  v_exp_f32 v20, v168\n"
    "  v_exp_f32 v21, v169\n"
    "  v_exp_f32 v22, v170\n"
    "  v_exp_f32 v23, v171\n"
    "  ds_read_b32 v11, v2 offset:1024\n"
    "  v_fma_f32 v16, v16, s54, s54\n"
    "  v_fma_f32 v17, v17, s54, s54\n"
    "  v_fma_f32 v18, v18, s54, s54\n"
    "  v_fma_f32 v19, v19, s54, s54\n"
    "  v_fma_f32 v20, v20, s54, s54\n"
    "  v_fma_f32 v21, v21, s54, s54\n"
    "  v_fma_f32 v22, v22, s54, s54\n"
    "  v_fma_f32 v23, v23, s54, s54\n"
    "  v_rcp_f32 v16, v16\n"
    "  v_rcp_f32 v17, v17\n"
    "  v_rcp_f32 v18, v18\n"
    "  v_rcp_f32 v19, v19\n"
    "  s_cmp_eq_u32 s35, 3\n"
    "  s_cbranch_scc1 .Ledm_b63\n"
    "  s_waitcnt lgkmcnt(0)\n"
    "  v_cmp_gt_u32_e32 vcc, 2, v11\n"
    "  s_branch .Ledm_f62\n"
    ".Ledm_b63:\n"
    "  v_rcp_f32 v20, v20\n"
    "  v_rcp_f32 v21, v21\n"
    "  v_rcp_f32 v22, v22\n"
    "  v_rcp_f32 v23, v23\n"
    "  v_mul_f32 v164, v164, v16\n"
    "  v_mul_f32 v165, v165, v17\n"
    "  v_mul_f32 v166, v166, v18\n"
    "  v_mul_f32 v167, v167, v19\n"
    "  v_mul_f32 v168, v168, v20\n"
    "  v_mul_f32 v169, v169, v21\n"
    "  v_mul_f32 v170, v170, v22\n"
    "  v_mul_f32 v171, v171, v23\n"
    "  v_cvt_pk_bf16_f32 v160, v164, v165\n"
    "  v_cvt_pk_bf16_f32 v161, v166, v167\n"
    "  v_cvt_pk_bf16_f32 v162, v168, v169\n"
    "  v_cvt_pk_bf16_f32 v163, v170, v171\n"
    "  v_fma_f32 v172, v172, s36, v88\n"
    "  v_fma_f32 v173, v173, s36, v89\n"
    "  ds_read_b32 v11, v2 offset:1024\n"
    "  v_fma_f32 v174, v174, s36, v90\n"
    "  v_fma_f32 v175, v175, s36, v91\n"
    "  v_exp_f32 v16, v172\n"
    "  v_exp_f32 v17, v173\n"
    "  v_exp_f32 v18, v174\n"
    "  v_exp_f32 v19, v175\n"
    "  v_fma_f32 v16, v16, s54, s54\n"
    "  v_fma_f32 v17, v17, s54, s54\n"
    "  v_fma_f32 v18, v18, s54, s54\n"
    "  v_fma_f32 v19, v19, s54, s54\n"
    "  v_rcp_f32 v16, v16\n"
    "  v_rcp_f32 v17, v17\n"
    "  v_rcp_f32 v18, v18\n"
    "  v_rcp_f32 v19, v19\n"
    "  v_mul_f32 v172, v172, v16\n"
    "  v_mul_f32 v173, v173, v17\n"
    "  v_mul_f32 v174, v174, v18\n"
    "  v_mul_f32 v175, v175, v19\n"
    "  v_cvt_pk_bf16_f32 v164, v172, v173\n"
    "  v_cvt_pk_bf16_f32 v165, v174, v175\n"
    "  s_waitcnt lgkmcnt(0)\n"
    "  v_cmp_gt_u32_e32 vcc, 2, v11\n"
    "  s_branch .Ledm_f65\n"
    ".Ledm_b66:\n"
    "  v_fma_f32 v176, v176, s36, v72\n"
    "  v_fma_f32 v177, v177, s36, v73\n"
    "  v_fma_f32 v178, v178, s36, v74\n"
    "  v_fma_f32 v179, v179, s36, v75\n"
    "  v_fma_f32 v180, v180, s36, v76\n"
    "  v_fma_f32 v181, v181, s36, v77\n"
    "  v_fma_f32 v182, v182, s36, v78\n"
    "  v_fma_f32 v183, v183, s36, v79\n"
    "  v_exp_f32 v16, v176\n"
    "  v_exp_f32 v17, v177\n"
    "  v_exp_f32 v18, v178\n"
    "  v_exp_f32 v19, v179\n"
    "  v_exp_f32 v20, v180\n"
    "  v_exp_f32 v21, v181\n"
    "  v_exp_f32 v22, v182\n"
    "  v_exp_f32 v23, v183\n"
    "  v_fma_f32 v16, v16, s54, s54\n"
    "  v_fma_f32 v17, v17, s54, s54\n"
    "  v_fma_f32 v18, v18, s54, s54\n"
    "  v_fma_f32 v19, v19, s54, s54\n"
    "  v_fma_f32 v20, v20, s54, s54\n"
    "  v_fma_f32 v21, v21, s54, s54\n"
    "  v_fma_f32 v22, v22, s54, s54\n"
    "  v_fma_f32 v23, v23, s54, s54\n"
    "  ds_read_b32 v11, v2 offset:1024\n"
    "  v_rcp_f32 v16, v16\n"
    "  v_rcp_f32 v17, v17\n"
    "  v_rcp_f32 v18, v18\n"
    "  v_rcp_f32 v19, v19\n"
    "  v_rcp_f32 v20, v20\n"
    "  v_rcp_f32 v21, v21\n"
    "  v_rcp_f32 v22, v22\n"
    "  v_rcp_f32 v23, v23\n"
    "  v_mul_f32 v176, v176, v16\n"
    "  v_mul_f32 v177, v177, v17\n"
    "  v_mul_f32 v178, v178, v18\n"
    "  v_mul_f32 v179, v179, v19\n"
    "  s_cmp_eq_u32 s35, 4\n"
    "  s_cbranch_scc1 .Ledm_b70\n"
    "  s_waitcnt lgkmcnt(0)\n"
    "  v_cmp_gt_u32_e32 vcc, 2, v11\n"
    "  s_branch .Ledm_f69\n"
    ".Ledm_b70:\n"
    "  v_mul_f32 v180, v180, v20\n"
    "  v_mul_f32 v181, v181, v21\n"
    "  v_mul_f32 v182, v182, v22\n"
    "  v_mul_f32 v183, v183, v23\n"
    "  v_cvt_pk_bf16_f32 v176, v176, v177\n"
    "  v_cvt_pk_bf16_f32 v177, v178, v179\n"
    "  v_cvt_pk_bf16_f32 v178, v180, v181\n"
    "  v_cvt_pk_bf16_f32 v179, v182, v183\n"
    "  v_fma_f32 v184, v184, s36, v80\n"
    "  v_fma_f32 v185, v185, s36, v81\n"
    "  v_fma_f32 v186, v186, s36, v82\n"
    "  v_fma_f32 v187, v187, s36, v83\n"
    "  v_fma_f32 v188, v188, s36, v84\n"
    "  v_fma_f32 v189, v189, s36, v85\n"
    "  v_fma_f32 v190, v190, s36, v86\n"
    "  v_fma_f32 v191, v191, s36, v87\n"
    "  v_exp_f32 v16, v184\n"
    "  v_exp_f32 v17, v185\n"
    "  v_exp_f32 v18, v186\n"
    "  v_exp_f32 v19, v187\n"
    "  v_exp_f32 v20, v188\n"
    "  v_exp_f32 v21, v189\n"
    "  v_exp_f32 v22, v190\n"
    "  v_exp_f32 v23, v191\n"
    "  ds_read_b32 v11, v2 offset:1024\n"
    "  v_fma_f32 v16, v16, s54, s54\n"
    "  v_fma_f32 v17, v17, s54, s54\n"
    "  v_fma_f32 v18, v18, s54, s54\n"
    "  v_fma_f32 v19, v19, s54, s54\n"
    "  v_fma_f32 v20, v20, s54, s54\n"
    "  v_fma_f32 v21, v21, s54, s54\n"
    "  v_fma_f32 v22, v22, s54, s54\n"
    "  v_fma_f32 v23, v23, s54, s54\n"
    "  v_rcp_f32 v16, v16\n"
    "  v_rcp_f32 v17, v17\n"
    "  v_rcp_f32 v18, v18\n"
    "  v_rcp_f32 v19, v19\n"
    "  s_cmp_eq_u32 s35, 4\n"
    "  s_cbranch_scc1 .Ledm_b74\n"
    "  s_waitcnt lgkmcnt(0)\n"
    "  v_cmp_gt_u32_e32 vcc, 2, v11\n"
    "  s_branch .Ledm_f73\n"
    ".Ledm_b74:\n"
    "  v_rcp_f32 v20, v20\n"
    "  v_rcp_f32 v21, v21\n"
    "  v_rcp_f32 v22, v22\n"
    "  v_rcp_f32 v23, v23\n"
    "  v_mul_f32 v184, v184, v16\n"
    "  v_mul_f32 v185, v185, v17\n"
    "  v_mul_f32 v186, v186, v18\n"
    "  v_mul_f32 v187, v187, v19\n"
    "  v_mul_f32 v188, v188, v20\n"
    "  v_mul_f32 v189, v189, v21\n"
    "  v_mul_f32 v190, v190, v22\n"
    "  v_mul_f32 v191, v191, v23\n"
    "  v_cvt_pk_bf16_f32 v180, v184, v185\n"
    "  v_cvt_pk_bf16_f32 v181, v186, v187\n"
    "  v_cvt_pk_bf16_f32 v182, v188, v189\n"
    "  v_cvt_pk_bf16_f32 v183, v190, v191\n"
    "  v_fma_f32 v192, v192, s36, v88\n"
    "  v_fma_f32 v193, v193, s36, v89\n"
    "  ds_read_b32 v11, v2 offset:1024\n"
    "  v_fma_f32 v194, v194, s36, v90\n"
    "  v_fma_f32 v195, v195, s36, v91\n"
    "  v_exp_f32 v16, v192\n"
    "  v_exp_f32 v17, v193\n"
    "  v_exp_f32 v18, v194\n"
    "  v_exp_f32 v19, v195\n"
    "  v_fma_f32 v16, v16, s54, s54\n"
    "  v_fma_f32 v17, v17, s54, s54\n"
    "  v_fma_f32 v18, v18, s54, s54\n"
    "  v_fma_f32 v19, v19, s54, s54\n"
    "  v_rcp_f32 v16, v16\n"
    "  v_rcp_f32 v17, v17\n"
    "  v_rcp_f32 v18, v18\n"
    "  v_rcp_f32 v19, v19\n"
    "  v_mul_f32 v192, v192, v16\n"
    "  v_mul_f32 v193, v193, v17\n"
    "  v_mul_f32 v194, v194, v18\n"
    "  v_mul_f32 v195, v195, v19\n"
    "  v_cvt_pk_bf16_f32 v184, v192, v193\n"
    "  v_cvt_pk_bf16_f32 v185, v194, v195\n"
    ".Ledm_pl79:\n"
    "  s_sub_u32 s39, s39, 1\n"
    "  s_cbranch_scc1 .Ledm_abort\n"
    "  s_waitcnt lgkmcnt(0)\n"
    "  ds_read_b32 v11, v2 offset:1024\n"
    "  s_waitcnt lgkmcnt(0)\n"
    "  v_cmp_gt_u32_e32 vcc, 2, v11\n"
    "  s_cbranch_vccnz .Ledm_pl79\n"
    "  s_branch .Ledm_f77\n"
    ".Ledm_b78:\n"
    "  v_fma_f32 v196, v196, s36, v72\n"
    "  v_fma_f32 v197, v197, s36, v73\n"
    "  v_fma_f32 v198, v198, s36, v74\n"
    "  v_fma_f32 v199, v199, s36, v75\n"
    "  v_fma_f32 v200, v200, s36, v76\n"
    "  v_fma_f32 v201, v201, s36, v77\n"
    "  v_fma_f32 v202, v202, s36, v78\n"
    "  v_fma_f32 v203, v203, s36, v79\n"
    "  v_exp_f32 v16, v196\n"
    "  v_exp_f32 v17, v197\n"
    "  v_exp_f32 v18, v198\n"
    "  v_exp_f32 v19, v199\n"
    "  v_exp_f32 v20, v200\n"
    "  v_exp_f32 v21, v201\n"
    "  v_exp_f32 v22, v202\n"
    "  v_exp_f32 v23, v203\n"
    "  v_fma_f32 v16, v16, s54, s54\n"
    "  v_fma_f32 v17, v17, s54, s54\n"
    "  v_fma_f32 v18, v18, s54, s54\n"
    "  v_fma_f32 v19, v19, s54, s54\n"
    "  v_fma_f32 v20, v20, s54, s54\n"
    "  v_fma_f32 v21, v21, s54, s54\n"
    "  v_fma_f32 v22, v22, s54, s54\n"
    "  v_fma_f32 v23, v23, s54, s54\n"
    "  v_rcp_f32 v16, v16\n"
    "  v_rcp_f32 v17, v17\n"
    "  v_rcp_f32 v18, v18\n"
    "  v_rcp_f32 v19, v19\n"
    "  v_rcp_f32 v20, v20\n"
    "  v_rcp_f32 v21, v21\n"
    "  v_rcp_f32 v22, v22\n"
    "  v_rcp_f32 v23, v23\n"
    "  v_mul_f32 v196, v196, v16\n"
    "  v_mul_f32 v197, v197, v17\n"
    "  v_mul_f32 v198, v198, v18\n"
    "  v_mul_f32 v199, v199, v19\n"
    "  v_mul_f32 v200, v200, v20\n"
    "  v_mul_f32 v201, v201, v21\n"
    "  v_mul_f32 v202, v202, v22\n"
    "  v_mul_f32 v203, v203, v23\n"
    "  v_cvt_pk_bf16_f32 v196, v196, v197\n"
    "  v_cvt_pk_bf16_f32 v197, v198, v199\n"
    "  v_cvt_pk_bf16_f32 v198, v200, v201\n"
    "  v_cvt_pk_bf16_f32 v199, v202, v203\n"
    "  v_fma_f32 v204, v204, s36, v80\n"
    "  v_fma_f32 v205, v205, s36, v81\n"
    "  v_fma_f32 v206, v206, s36, v82\n"
    "  v_fma_f32 v207, v207, s36, v83\n"
    "  v_fma_f32 v208, v208, s36, v84\n"
    "  v_fma_f32 v209, v209, s36, v85\n"
    "  v_fma_f32 v210, v210, s36, v86\n"
    "  v_fma_f32 v211, v211, s36, v87\n"
    "  v_exp_f32 v16, v204\n"
    "  v_exp_f32 v17, v205\n"
    "  v_exp_f32 v18, v206\n"
    "  v_exp_f32 v19, v207\n"
    "  v_exp_f32 v20, v208\n"
    "  v_exp_f32 v21, v209\n"
    "  v_exp_f32 v22, v210\n"
    "  v_exp_f32 v23, v211\n"
    "  v_fma_f32 v16, v16, s54, s54\n"
    "  v_fma_f32 v17, v17, s54, s54\n"
    "  v_fma_f32 v18, v18, s54, s54\n"
    "  v_fma_f32 v19, v19, s54, s54\n"
    "  v_fma_f32 v20, v20, s54, s54\n"
    "  v_fma_f32 v21, v21, s54, s54\n"
    "  v_fma_f32 v22, v22, s54, s54\n"
    "  v_fma_f32 v23, v23, s54, s54\n"
    "  v_rcp_f32 v16, v16\n"
    "  v_rcp_f32 v17, v17\n"
    "  v_rcp_f32 v18, v18\n"
    "  v_rcp_f32 v19, v19\n"
    "  v_rcp_f32 v20, v20\n"
    "  v_rcp_f32 v21, v21\n"
    "  v_rcp_f32 v22, v22\n"
    "  v_rcp_f32 v23, v23\n"
    "  v_mul_f32 v204, v204, v16\n"
    "  v_mul_f32 v205, v205, v17\n"
    "  v_mul_f32 v206, v206, v18\n"
    "  v_mul_f32 v207, v207, v19\n"
    "  v_mul_f32 v208, v208, v20\n"
    "  v_mul_f32 v209, v209, v21\n"
    "  v_mul_f32 v210, v210, v22\n"
    "  v_mul_f32 v211, v211, v23\n"
    "  v_cvt_pk_bf16_f32 v200, v204, v205\n"
    "  v_cvt_pk_bf16_f32 v201, v206, v207\n"
    "  v_cvt_pk_bf16_f32 v202, v208, v209\n"
    "  v_cvt_pk_bf16_f32 v203, v210, v211\n"
    "  v_fma_f32 v212, v212, s36, v88\n"
    "  v_fma_f32 v213, v213, s36, v89\n"
    "  v_fma_f32 v214, v214, s36, v90\n"
    "  v_fma_f32 v215, v215, s36, v91\n"
    "  v_exp_f32 v16, v212\n"
    "  v_exp_f32 v17, v213\n"
    "  v_exp_f32 v18, v214\n"
    "  v_exp_f32 v19, v215\n"
    "  v_fma_f32 v16, v16, s54, s54\n"
    "  v_fma_f32 v17, v17, s54, s54\n"
    "  v_fma_f32 v18, v18, s54, s54\n"
    "  v_fma_f32 v19, v19, s54, s54\n"
    "  v_rcp_f32 v16, v16\n"
    "  v_rcp_f32 v17, v17\n"
    "  v_rcp_f32 v18, v18\n"
    "  v_rcp_f32 v19, v19\n"
    "  v_mul_f32 v212, v212, v16\n"
    "  v_mul_f32 v213, v213, v17\n"
    "  v_mul_f32 v214, v214, v18\n"
    "  v_mul_f32 v215, v215, v19\n"
    "  v_cvt_pk_bf16_f32 v204, v212, v213\n"
    "  v_cvt_pk_bf16_f32 v205, v214, v215\n"
    "  ds_write_b128 v8, v[196:199] offset:20480\n"
    "  ds_write_b128 v8, v[200:203] offset:21504\n"
    "  ds_write_b64 v9, v[204:205] offset:20480\n"
    "  ds_add_u32 v10, v15 offset:0\n"
    "  v_fma_f32 v216, v216, s36, v72\n"
    "  v_fma_f32 v217, v217, s36, v73\n"
    "  v_fma_f32 v218, v218, s36, v74\n"
    "  v_fma_f32 v219, v219, s36, v75\n"
    "  v_fma_f32 v220, v220, s36, v76\n"
    "  v_fma_f32 v221, v221, s36, v77\n"
    "  v_fma_f32 v222, v222, s36, v78\n"
    "  v_fma_f32 v223, v223, s36, v79\n"
    "  v_exp_f32 v16, v216\n"
    "  v_exp_f32 v17, v217\n"
    "  v_exp_f32 v18, v218\n"
    "  v_exp_f32 v19, v219\n"
    "  v_exp_f32 v20, v220\n"
    "  v_exp_f32 v21, v221\n"
    "  v_exp_f32 v22, v222\n"
    "  v_exp_f32 v23, v223\n"
    "  v_fma_f32 v16, v16, s54, s54\n"
    "  v_fma_f32 v17, v17, s54, s54\n"
    "  v_fma_f32 v18, v18, s54, s54\n"
    "  v_fma_f32 v19, v19, s54, s54\n"
    "  v_fma_f32 v20, v20, s54, s54\n"
    "  v_fma_f32 v21, v21, s54, s54\n"
    "  v_fma_f32 v22, v22, s54, s54\n"
    "  v_fma_f32 v23, v23, s54, s54\n"
    "  v_rcp_f32 v16, v16\n"
    "  v_rcp_f32 v17, v17\n"
    "  v_rcp_f32 v18, v18\n"
    "  v_rcp_f32 v19, v19\n"
    "  v_rcp_f32 v20, v20\n"
    "  v_rcp_f32 v21, v21\n"
    "  v_rcp_f32 v22, v22\n"
    "  v_rcp_f32 v23, v23\n"
    "  v_mul_f32 v216, v216, v16\n"
    "  v_mul_f32 v217, v217, v17\n"
    "  v_mul_f32 v218, v218, v18\n"
    "  v_mul_f32 v219, v219, v19\n"
    "  v_mul_f32 v220, v220, v20\n"
    "  v_mul_f32 v221, v221, v21\n"
    "  v_mul_f32 v222, v222, v22\n"
    "  v_mul_f32 v223, v223, v23\n"
    "  v_cvt_pk_bf16_f32 v216, v216, v217\n"
    "  v_cvt_pk_bf16_f32 v217, v218, v219\n"
    "  v_cvt_pk_bf16_f32 v218, v220, v221\n"
    "  v_cvt_pk_bf16_f32 v219, v222, v223\n"
    "  v_fma_f32 v224, v224, s36, v80\n"
    "  v_fma_f32 v225, v225, s36, v81\n"
    "  v_fma_f32 v226, v226, s36, v82\n"
    "  v_fma_f32 v227, v227, s36, v83\n"
    "  v_fma_f32 v228, v228, s36, v84\n"
    "  v_fma_f32 v229, v229, s36, v85\n"
    "  v_fma_f32 v230, v230, s36, v86\n"
    "  v_fma_f32 v231, v231, s36, v87\n"
    "  v_exp_f32 v16, v224\n"
    "  v_exp_f32 v17, v225\n"
    "  v_exp_f32 v18, v226\n"
    "  v_exp_f32 v19, v227\n"
    "  v_exp_f32 v20, v228\n"
    "  v_exp_f32 v21, v229\n"
    "  v_exp_f32 v22, v230\n"
    "  v_exp_f32 v23, v231\n"
    "  v_fma_f32 v16, v16, s54, s54\n"
    "  v_fma_f32 v17, v17, s54, s54\n"
    "  v_fma_f32 v18, v18, s54, s54\n"
    "  v_fma_f32 v19, v19, s54, s54\n"
    "  v_fma_f32 v20, v20, s54, s54\n"
    "  v_fma_f32 v21, v21, s54, s54\n"
    "  v_fma_f32 v22, v22, s54, s54\n"
    "  v_fma_f32 v23, v23, s54, s54\n"
    "  v_rcp_f32 v16, v16\n"
    "  v_rcp_f32 v17, v17\n"
    "  v_rcp_f32 v18, v18\n"
    "  v_rcp_f32 v19, v19\n"
    "  v_rcp_f32 v20, v20\n"
    "  v_rcp_f32 v21, v21\n"
    "  v_rcp_f32 v22, v22\n"
    "  v_rcp_f32 v23, v23\n"
    "  v_mul_f32 v224, v224, v16\n"
    "  v_mul_f32 v225, v225, v17\n"
    "  v_mul_f32 v226, v226, v18\n"
    "  v_mul_f32 v227, v227, v19\n"
    "  v_mul_f32 v228, v228, v20\n"
    "  v_mul_f32 v229, v229, v21\n"
    "  v_mul_f32 v230, v230, v22\n"
    "  v_mul_f32 v231, v231, v23\n"
    "  v_cvt_pk_bf16_f32 v220, v224, v225\n"
    "  v_cvt_pk_bf16_f32 v221, v226, v227\n"
    "  v_cvt_pk_bf16_f32 v222, v228, v229\n"
    "  v_cvt_pk_bf16_f32 v223, v230, v231\n"
    "  v_fma_f32 v232, v232, s36, v88\n"
    "  v_fma_f32 v233, v233, s36, v89\n"
    "  v_fma_f32 v234, v234, s36, v90\n"
    "  v_fma_f32 v235, v235, s36, v91\n"
    "  v_exp_f32 v16, v232\n"
    "  v_exp_f32 v17, v233\n"
    "  v_exp_f32 v18, v234\n"
    "  v_exp_f32 v19, v235\n"
    "  v_fma_f32 v16, v16, s54, s54\n"
    "  v_fma_f32 v17, v17, s54, s54\n"
    "  v_fma_f32 v18, v18, s54, s54\n"
    "  v_fma_f32 v19, v19, s54, s54\n"
    "  v_rcp_f32 v16, v16\n"
    "  v_rcp_f32 v17, v17\n"
    "  v_rcp_f32 v18, v18\n"
    "  v_rcp_f32 v19, v19\n"
    "  v_mul_f32 v232, v232, v16\n"
    "  v_mul_f32 v233, v233, v17\n"
    "  v_mul_f32 v234, v234, v18\n"
    "  v_mul_f32 v235, v235, v19\n"
    "  v_cvt_pk_bf16_f32 v224, v232, v233\n"
    "  v_cvt_pk_bf16_f32 v225, v234, v235\n"
    "  ds_write_b128 v8, v[216:219] offset:30720\n"
    "  ds_write_b128 v8, v[220:223] offset:31744\n"
    "  ds_write_b64 v9, v[224:225] offset:30720\n"
    "  ds_add_u32 v10, v15 offset:0\n"
    "  v_fma_f32 v236, v236, s36, v72\n"
    "  v_fma_f32 v237, v237, s36, v73\n"
    "  v_fma_f32 v238, v238, s36, v74\n"
    "  v_fma_f32 v239, v239, s36, v75\n"
    "  v_fma_f32 v240, v240, s36, v76\n"
    "  v_fma_f32 v241, v241, s36, v77\n"
    "  v_fma_f32 v242, v242, s36, v78\n"
    "  v_fma_f32 v243, v243, s36, v79\n"
    "  v_exp_f32 v16, v236\n"
    "  v_exp_f32 v17, v237\n"
    "  v_exp_f32 v18, v238\n"
    "  v_exp_f32 v19, v239\n"
    "  v_exp_f32 v20, v240\n"
    "  v_exp_f32 v21, v241\n"
    "  v_exp_f32 v22, v242\n"
    "  v_exp_f32 v23, v243\n"
    "  v_fma_f32 v16, v16, s54, s54\n"
    "  v_fma_f32 v17, v17, s54, s54\n"
    "  v_fma_f32 v18, v18, s54, s54\n"
    "  v_fma_f32 v19, v19, s54, s54\n"
    "  v_fma_f32 v20, v20, s54, s54\n"
    "  v_fma_f32 v21, v21, s54, s54\n"
    "  v_fma_f32 v22, v22, s54, s54\n"
    "  v_fma_f32 v23, v23, s54, s54\n"
    "  v_rcp_f32 v16, v16\n"
    "  v_rcp_f32 v17, v17\n"
    "  v_rcp_f32 v18, v18\n"
    "  v_rcp_f32 v19, v19\n"
    "  v_rcp_f32 v20, v20\n"
    "  v_rcp_f32 v21, v21\n"
    "  v_rcp_f32 v22, v22\n"
    "  v_rcp_f32 v23, v23\n"
    "  v_mul_f32 v236, v236, v16\n"
    "  v_mul_f32 v237, v237, v17\n"
    "  v_mul_f32 v238, v238, v18\n"
    "  v_mul_f32 v239, v239, v19\n"
    "  v_mul_f32 v240, v240, v20\n"
    "  v_mul_f32 v241, v241, v21\n"
    "  v_mul_f32 v242, v242, v22\n"
    "  v_mul_f32 v243, v243, v23\n"
    "  v_cvt_pk_bf16_f32 v236, v236, v237\n"
    "  v_cvt_pk_bf16_f32 v237, v238, v239\n"
    "  v_cvt_pk_bf16_f32 v238, v240, v241\n"
    "  v_cvt_pk_bf16_f32 v239, v242, v243\n"
    "  v_fma_f32 v244, v244, s36, v80\n"
    "  v_fma_f32 v245, v245, s36, v81\n"
    "  v_fma_f32 v246, v246, s36, v82\n"
    "  v_fma_f32 v247, v247, s36, v83\n"
    "  v_fma_f32 v248, v248, s36, v84\n"
    "  v_fma_f32 v249, v249, s36, v85\n"
    "  v_fma_f32 v250, v250, s36, v86\n"
    "  v_fma_f32 v251, v251, s36, v87\n"
    "  v_exp_f32 v16, v244\n"
    "  v_exp_f32 v17, v245\n"
    "  v_exp_f32 v18, v246\n"
    "  v_exp_f32 v19, v247\n"
    "  v_exp_f32 v20, v248\n"
    "  v_exp_f32 v21, v249\n"
    "  v_exp_f32 v22, v250\n"
    "  v_exp_f32 v23, v251\n"
    "  v_fma_f32 v16, v16, s54, s54\n"
    "  v_fma_f32 v17, v17, s54, s54\n"
    "  v_fma_f32 v18, v18, s54, s54\n"
    "  v_fma_f32 v19, v19, s54, s54\n"
    "  v_fma_f32 v20, v20, s54, s54\n"
    "  v_fma_f32 v21, v21, s54, s54\n"
    "  v_fma_f32 v22, v22, s54, s54\n"
    "  v_fma_f32 v23, v23, s54, s54\n"
    "  v_rcp_f32 v16, v16\n"
    "  v_rcp_f32 v17, v17\n"
    "  v_rcp_f32 v18, v18\n"
    "  v_rcp_f32 v19, v19\n"
    "  v_rcp_f32 v20, v20\n"
    "  v_rcp_f32 v21, v21\n"
    "  v_rcp_f32 v22, v22\n"
    "  v_rcp_f32 v23, v23\n"
    "  v_mul_f32 v244, v244, v16\n"
    "  v_mul_f32 v245, v245, v17\n"
    "  v_mul_f32 v246, v246, v18\n"
    "  v_mul_f32 v247, v247, v19\n"
    "  v_mul_f32 v248, v248, v20\n"
    "  v_mul_f32 v249, v249, v21\n"
    "  v_mul_f32 v250, v250, v22\n"
    "  v_mul_f32 v251, v251, v23\n"
    "  v_cvt_pk_bf16_f32 v240, v244, v245\n"
    "  v_cvt_pk_bf16_f32 v241, v246, v247\n"
    "  v_cvt_pk_bf16_f32 v242, v248, v249\n"
    "  v_cvt_pk_bf16_f32 v243, v250, v251\n"
    "  v_fma_f32 v252, v252, s36, v88\n"
    "  v_fma_f32 v253, v253, s36, v89\n"
    "  ds_read_b32 v11, v2 offset:1024\n"
    "  v_fma_f32 v254, v254, s36, v90\n"
    "  v_fma_f32 v255, v255, s36, v91\n"
    "  v_exp_f32 v16, v252\n"
    "  v_exp_f32 v17, v253\n"
    "  v_exp_f32 v18, v254\n"
    "  v_exp_f32 v19, v255\n"
    "  v_fma_f32 v16, v16, s54, s54\n"
    "  v_fma_f32 v17, v17, s54, s54\n"
    "  v_fma_f32 v18, v18, s54, s54\n"
    "  v_fma_f32 v19, v19, s54, s54\n"
    "  v_rcp_f32 v16, v16\n"
    "  v_rcp_f32 v17, v17\n"
    "  v_rcp_f32 v18, v18\n"
    "  v_rcp_f32 v19, v19\n"
    "  v_mul_f32 v252, v252, v16\n"
    "  v_mul_f32 v253, v253, v17\n"
    "  v_mul_f32 v254, v254, v18\n"
    "  v_mul_f32 v255, v255, v19\n"
    "  v_cvt_pk_bf16_f32 v244, v252, v253\n"
    "  v_cvt_pk_bf16_f32 v245, v254, v255\n"
    "  s_waitcnt lgkmcnt(0)\n"
    "  v_cmp_gt_u32_e32 vcc, 2, v11\n"
    "  s_cbranch_vccnz .Ledm_sl83\n"
    ".Ledm_bk84:\n"
    "  ds_write_b128 v8, v[236:239] offset:40960\n"
    "  ds_write_b128 v8, v[240:243] offset:41984\n"
    "  ds_write_b64 v9, v[244:245] offset:40960\n"
    "  ds_add_u32 v10, v15 offset:0\n"
    "  s_endpgm\n"
    ".Ledm_sl36:\n"
    "  s_sub_u32 s39, s39, 1\n"
    "  s_cbranch_scc1 .Ledm_abort\n"
    "  ds_read_b32 v11, v15 offset:0\n"
    "  s_waitcnt lgkmcnt(0)\n"
    "  v_cmp_gt_u32_e32 vcc, 0xb0000004, v11\n"
    "  s_cbranch_vccnz .Ledm_sl36\n"
    "  s_branch .Ledm_bk37\n"
    ".Ledm_sl38:\n"
    "  s_sub_u32 s39, s39, 1\n"
    "  s_cbranch_scc1 .Ledm_abort\n"
    "  ds_read_b32 v11, v15 offset:0\n"
    "  s_waitcnt lgkmcnt(0)\n"
    "  v_cmp_gt_u32_e32 vcc, 0xb0000006, v11\n"
    "  s_cbranch_vccnz .Ledm_sl38\n"
    "  s_branch .Ledm_bk39\n"
    ".Ledm_sl40:\n"
    "  s_sub_u32 s39, s39, 1\n"
    "  s_cbranch_scc1 .Ledm_abort\n"
    "  ds_read_b32 v11, v15 offset:0\n"
    "  s_waitcnt lgkmcnt(0)\n"
    "  v_cmp_gt_u32_e32 vcc, 0xb0000008, v11\n"
    "  s_cbranch_vccnz .Ledm_sl40\n"
    "  s_branch .Ledm_bk41\n"
    ".Ledm_sl42:\n"
    "  s_sub_u32 s39, s39, 1\n"
    "  s_cbranch_scc1 .Ledm_abort\n"
    "  ds_read_b32 v11, v15 offset:0\n"
    "  s_waitcnt lgkmcnt(0)\n"
    "  v_cmp_gt_u32_e32 vcc, 0xb000000a, v11\n"
    "  s_cbranch_vccnz .Ledm_sl42\n"
    "  s_branch .Ledm_bk43\n"
    ".Ledm_sl44:\n"
    "  s_sub_u32 s39, s39, 1\n"
    "  s_cbranch_scc1 .Ledm_abort\n"
    "  ds_read_b32 v11, v15 offset:0\n"
    "  s_waitcnt lgkmcnt(0)\n"
    "  v_cmp_gt_u32_e32 vcc, 0xb000000c, v11\n"
    "  s_cbranch_vccnz .Ledm_sl44\n"
    "  s_branch .Ledm_bk45\n"
    ".Ledm_sl46:\n"
    "  s_sub_u32 s39, s39, 1\n"
    "  s_cbranch_scc1 .Ledm_abort\n"
    "  ds_read_b32 v11, v15 offset:0\n"
    "  s_waitcnt lgkmcnt(0)\n"
    "  v_cmp_gt_u32_e32 vcc, 0xb000000e, v11\n"
    "  s_cbranch_vccnz .Ledm_sl46\n"
    "  s_branch .Ledm_bk47\n"
    ".Ledm_sl48:\n"
    "  s_sub_u32 s39, s39, 1\n"
    "  s_cbranch_scc1 .Ledm_abort\n"
    "  ds_read_b32 v11, v15 offset:0\n"
    "  s_waitcnt lgkmcnt(0)\n"
    "  v_cmp_gt_u32_e32 vcc, 0xb0000010, v11\n"
    "  s_cbranch_vccnz .Ledm_sl48\n"
    "  s_branch .Ledm_bk49\n"
    ".Ledm_sl50:\n"
    "  s_sub_u32 s39, s39, 1\n"
    "  s_cbranch_scc1 .Ledm_abort\n"
    "  ds_read_b32 v11, v15 offset:0\n"
    "  s_waitcnt lgkmcnt(0)\n"
    "  v_cmp_gt_u32_e32 vcc, 0xb0000012, v11\n"
    "  s_cbranch_vccnz .Ledm_sl50\n"
    "  s_branch .Ledm_bk51\n"
    ".Ledm_sl52:\n"
    "  s_sub_u32 s39, s39, 1\n"
    "  s_cbranch_scc1 .Ledm_abort\n"
    "  ds_read_b32 v11, v15 offset:0\n"
    "  s_waitcnt lgkmcnt(0)\n"
    "  v_cmp_gt_u32_e32 vcc, 0xb0000014, v11\n"
    "  s_cbranch_vccnz .Ledm_sl52\n"
    "  s_branch .Ledm_bk53\n"
    ".Ledm_sl54:\n"
    "  s_sub_u32 s39, s39, 1\n"
    "  s_cbranch_scc1 .Ledm_abort\n"
    "  ds_read_b32 v11, v2 offset:0\n"
    "  s_waitcnt lgkmcnt(0)\n"
    "  v_cmp_gt_u32_e32 vcc, 20, v11\n"
    "  s_cbranch_vccnz .Ledm_sl54\n"
    "  s_branch .Ledm_bk55\n"
    ".Ledm_f56:\n"
    "  s_and_b64 s[46:47], vcc, s[44:45]\n"
    "  s_cbranch_scc1 .Ledm_b57\n"
    "  ds_write_b128 v8, v[136:139] offset:61440\n"
    "  ds_write_b128 v8, v[140:143] offset:62464\n"
    "  ds_write_b64 v9, v[144:145] offset:61440\n"
    "  ds_add_u32 v10, v15 offset:0\n"
    "  s_mov_b32 s35, 3\n"
    ".Ledm_q58:\n"
    "  s_branch .Ledm_b57\n"
    ".Ledm_f59:\n"
    "  s_and_b64 s[46:47], vcc, s[44:45]\n"
    "  s_cbranch_scc1 .Ledm_b60\n"
    "  ds_write_b128 v8, v[136:139] offset:61440\n"
    "  ds_write_b128 v8, v[140:143] offset:62464\n"
    "  ds_write_b64 v9, v[144:145] offset:61440\n"
    "  ds_add_u32 v10, v15 offset:0\n"
    "  s_mov_b32 s35, 3\n"
    ".Ledm_q61:\n"
    "  s_branch .Ledm_b60\n"
    ".Ledm_f62:\n"
    "  s_and_b64 s[46:47], vcc, s[44:45]\n"
    "  s_cbranch_scc1 .Ledm_b63\n"
    "  ds_write_b128 v8, v[136:139] offset:61440\n"
    "  ds_write_b128 v8, v[140:143] offset:62464\n"
    "  ds_write_b64 v9, v[144:145] offset:61440\n"
    "  ds_add_u32 v10, v15 offset:0\n"
    "  s_mov_b32 s35, 3\n"
    ".Ledm_q64:\n"
    "  s_branch .Ledm_b63\n"
    ".Ledm_f65:\n"
    "  s_cmp_lg_u32 s35, 2\n"
    "  s_cbranch_scc1 .Ledm_q67\n"
    "  s_and_b64 s[46:47], vcc, s[44:45]\n"
    "  s_cbranch_scc1 .Ledm_b66\n"
    "  ds_write_b128 v8, v[136:139] offset:61440\n"
    "  ds_write_b128 v8, v[140:143] offset:62464\n"
    "  ds_write_b64 v9, v[144:145] offset:61440\n"
    "  s_waitcnt lgkmcnt(14)\n"
    "  ds_add_u32 v10, v15 offset:0\n"
    "  s_mov_b32 s35, 3\n"
    ".Ledm_q67:\n"
    "  s_and_b64 s[46:47], vcc, s[44:45]\n"
    "  s_cbranch_scc1 .Ledm_b66\n"
    "  s_waitcnt lgkmcnt(14)\n"
    "  ds_write_b128 v8, v[156:159] offset:0\n"
    "  s_waitcnt lgkmcnt(14)\n"
    "  ds_write_b128 v8, v[160:163] offset:1024\n"
    "  s_waitcnt lgkmcnt(14)\n"
    "  ds_write_b64 v9, v[164:165] offset:0\n"
    "  s_waitcnt lgkmcnt(14)\n"
    "  ds_add_u32 v10, v15 offset:0\n"
    "  s_mov_b32 s35, 4\n"
    ".Ledm_q68:\n"
    "  s_branch .Ledm_b66\n"
    ".Ledm_f69:\n"
    "  s_cmp_lg_u32 s35, 2\n"
    "  s_cbranch_scc1 .Ledm_q71\n"
    "  s_and_b64 s[46:47], vcc, s[44:45]\n"
    "  s_cbranch_scc1 .Ledm_b70\n"
    "  s_waitcnt lgkmcnt(14)\n"
    "  ds_write_b128 v8, v[136:139] offset:61440\n"
    "  s_waitcnt lgkmcnt(14)\n"
    "  ds_write_b128 v8, v[140:143] offset:62464\n"
    "  s_waitcnt lgkmcnt(14)\n"
    "  ds_write_b64 v9, v[144:145] offset:61440\n"
    "  s_waitcnt lgkmcnt(14)\n"
    "  ds_add_u32 v10, v15 offset:0\n"
    "  s_mov_b32 s35, 3\n"
    ".Ledm_q71:\n"
    "  s_and_b64 s[46:47], vcc, s[44:45]\n"
    "  s_cbranch_scc1 .Ledm_b70\n"
    "  s_waitcnt lgkmcnt(14)\n"
    "  ds_write_b128 v8, v[156:159] offset:0\n"
    "  s_waitcnt lgkmcnt(14)\n"
    "  ds_write_b128 v8, v[160:163] offset:1024\n"
    "  s_waitcnt lgkmcnt(14)\n"
    "  ds_write_b64 v9, v[164:165] offset:0\n"
    "  s_waitcnt lgkmcnt(14)\n"
    "  ds_add_u32 v10, v15 offset:0\n"
    "  s_mov_b32 s35, 4\n"
    ".Ledm_q72:\n"
    "  s_branch .Ledm_b70\n"
    ".Ledm_f73:\n"
    "  s_cmp_lg_u32 s35, 2\n"
    "  s_cbranch_scc1 .Ledm_q75\n"
    "  s_and_b64 s[46:47], vcc, s[44:45]\n"
    "  s_cbranch_scc1 .Ledm_b74\n"
    "  s_waitcnt lgkmcnt(14)\n"
    "  ds_write_b128 v8, v[136:139] offset:61440\n"
    "  s_waitcnt lgkmcnt(14)\n"
    "  ds_write_b128 v8, v[140:143] offset:62464\n"
    "  s_waitcnt lgkmcnt(14)\n"
    "  ds_write_b64 v9, v[144:145] offset:61440\n"
    "  s_waitcnt lgkmcnt(14)\n"
    "  ds_add_u32 v10, v15 offset:0\n"
    "  s_mov_b32 s35, 3\n"
    ".Ledm_q75:\n"
    "  s_and_b64 s[46:47], vcc, s[44:45]\n"
    "  s_cbranch_scc1 .Ledm_b74\n"
    "  s_waitcnt lgkmcnt(14)\n"
    "  ds_write_b128 v8, v[156:159] offset:0\n"
    "  s_waitcnt lgkmcnt(14)\n"
    "  ds_write_b128 v8, v[160:163] offset:1024\n"
    "  s_waitcnt lgkmcnt(14)\n"
    "  ds_write_b64 v9, v[164:165] offset:0\n"
    "  s_waitcnt lgkmcnt(14)\n"
    "  ds_add_u32 v10, v15 offset:0\n"
    "  s_mov_b32 s35, 4\n"
    ".Ledm_q76:\n"
    "  s_branch .Ledm_b74\n"
    ".Ledm_f77:\n"
    "  s_cmp_lg_u32 s35, 2\n"
    "  s_cbranch_scc1 .Ledm_q80\n"
    "  s_and_b64 s[46:47], vcc, s[44:45]\n"
    "  s_cbranch_scc1 .Ledm_b78\n"
    "  s_waitcnt lgkmcnt(14)\n"
    "  ds_write_b128 v8, v[136:139] offset:61440\n"
    "  s_waitcnt lgkmcnt(14)\n"
    "  ds_write_b128 v8, v[140:143] offset:62464\n"
    "  s_waitcnt lgkmcnt(14)\n"
    "  ds_write_b64 v9, v[144:145] offset:61440\n"
    "  s_waitcnt lgkmcnt(14)\n"
    "  ds_add_u32 v10, v15 offset:0\n"
    "  s_mov_b32 s35, 3\n"
    ".Ledm_q80:\n"
    "  s_cmp_lg_u32 s35, 3\n"
    "  s_cbranch_scc1 .Ledm_q81\n"
    "  s_and_b64 s[46:47], vcc, s[44:45]\n"
    "  s_cbranch_scc1 .Ledm_b78\n"
    "  s_waitcnt lgkmcnt(14)\n"
    "  ds_write_b128 v8, v[156:159] offset:0\n"
    "  s_waitcnt lgkmcnt(14)\n"
    "  ds_write_b128 v8, v[160:163] offset:1024\n"
    "  s_waitcnt lgkmcnt(14)\n"
    "  ds_write_b64 v9, v[164:165] offset:0\n"
    "  s_waitcnt lgkmcnt(14)\n"
    "  ds_add_u32 v10, v15 offset:0\n"
    "  s_mov_b32 s35, 4\n"
    ".Ledm_q81:\n"
    "  s_cbranch_vccnz .Ledm_b78\n"
    "  s_waitcnt lgkmcnt(14)\n"
    "  ds_write_b128 v8, v[176:179] offset:10240\n"
    "  s_waitcnt lgkmcnt(14)\n"
    "  ds_write_b128 v8, v[180:183] offset:11264\n"
    "  s_waitcnt lgkmcnt(14)\n"
    "  ds_write_b64 v9, v[184:185] offset:10240\n"
    "  s_waitcnt lgkmcnt(14)\n"
    "  ds_add_u32 v10, v15 offset:0\n"
    "  s_mov_b32 s35, 5\n"
    ".Ledm_q82:\n"
    "  s_branch .Ledm_b78\n"
    ".Ledm_sl83:\n"
    "  s_sub_u32 s39, s39, 1\n"
    "  s_cbranch_scc1 .Ledm_abort\n"
    "  s_waitcnt lgkmcnt(14)\n"
    "  ds_read_b32 v11, v2 offset:1024\n"
    "  s_waitcnt lgkmcnt(0)\n"
    "  v_cmp_gt_u32_e32 vcc, 2, v11\n"
    "  s_cbranch_vccnz .Ledm_sl83\n"
    "  s_branch .Ledm_bk84\n"
    ".Ledm_Lm:\n"
    "  s_setprio 0\n"
    "  s_add_u32 s30, s30, 10240\n"
    "  s_addc_u32 s31, s31, 0\n"
    "  s_mov_b32 m0, 40960\n"
    "  s_nop 0\n"
    "  global_load_lds_dwordx4 v10, s[30:31] offset:0\n"
    "  global_load_lds_dwordx4 v11, s[30:31] offset:1024\n"
    "  global_load_lds_dwordx4 v12, s[30:31] offset:2048\n"
    "  global_load_lds_dwordx4 v13, s[30:31] offset:3072\n"
    "  s_mov_b32 m0, 45056\n"
    "  s_nop 0\n"
    "  global_load_lds_dwordx4 v14, s[30:31] offset:0\n"
    "  global_load_lds_dwordx4 v15, s[30:31] offset:1024\n"
    "  global_load_lds_dwordx4 v16, s[30:31] offset:2048\n"
    "  global_load_lds_dwordx4 v17, s[30:31] offset:3072\n"
    "  s_mov_b32 m0, 49152\n"
    "  s_nop 0\n"
    "  global_load_lds_dwordx4 v18, s[30:31] offset:0\n"
    "  global_load_lds_dwordx4 v19, s[30:31] offset:1024\n"
    "  v_mov_b32 v38, 20\n"
    "  s_mov_b32 m0, 156160\n"
    "  s_nop 0\n"
    "  global_load_lds_dword v38, s[52:53]\n"
    "  s_add_u32 s44, s44, 1048576\n"
    "  s_addc_u32 s45, s45, 0\n"
    "  s_mov_b32 m0, 88064\n"
    "  s_nop 0\n"
    "  global_load_lds_dwordx4 v30, s[44:45] offset:0\n"
    "  global_load_lds_dwordx4 v31, s[44:45] offset:1024\n"
    "  global_load_lds_dwordx4 v32, s[44:45] offset:2048\n"
    "  global_load_lds_dwordx4 v33, s[44:45] offset:3072\n"
    "  s_mov_b32 m0, 92160\n"
    "  s_nop 0\n"
    "  global_load_lds_dwordx4 v34, s[44:45] offset:0\n"
    "  global_load_lds_dwordx4 v35, s[44:45] offset:1024\n"
    "  global_load_lds_dwordx4 v36, s[44:45] offset:2048\n"
    "  global_load_lds_dwordx4 v37, s[44:45] offset:3072\n"
    "  v_mov_b32 v38, 28\n"
    "  s_mov_b32 m0, 156416\n"
    "  s_nop 0\n"
    "  global_load_lds_dword v38, s[52:53]\n"
    "  s_add_u32 s30, s30, 10240\n"
    "  s_addc_u32 s31, s31, 0\n"
    "  s_mov_b32 m0, 51200\n"
    "  s_nop 0\n"
    "  global_load_lds_dwordx4 v10, s[30:31] offset:0\n"
    "  global_load_lds_dwordx4 v11, s[30:31] offset:1024\n"
    "  global_load_lds_dwordx4 v12, s[30:31] offset:2048\n"
    "  global_load_lds_dwordx4 v13, s[30:31] offset:3072\n"
    "  s_mov_b32 m0, 55296\n"
    "  s_nop 0\n"
    "  global_load_lds_dwordx4 v14, s[30:31] offset:0\n"
    "  global_load_lds_dwordx4 v15, s[30:31] offset:1024\n"
    "  global_load_lds_dwordx4 v16, s[30:31] offset:2048\n"
    "  global_load_lds_dwordx4 v17, s[30:31] offset:3072\n"
    "  s_mov_b32 m0, 59392\n"
    "  s_nop 0\n"
    "  global_load_lds_dwordx4 v18, s[30:31] offset:0\n"
    "  global_load_lds_dwordx4 v19, s[30:31] offset:1024\n"
    "  v_mov_b32 v38, 24\n"
    "  s_mov_b32 m0, 156160\n"
    "  s_nop 0\n"
    "  global_load_lds_dword v38, s[52:53]\n"
    "  s_add_u32 s30, s30, 10240\n"
    "  s_addc_u32 s31, s31, 0\n"
    "  s_mov_b32 m0, 61440\n"
    "  s_nop 0\n"
    "  global_load_lds_dwordx4 v10, s[30:31] offset:0\n"
    "  global_load_lds_dwordx4 v11, s[30:31] offset:1024\n"
    "  global_load_lds_dwordx4 v12, s[30:31] offset:2048\n"
    "  global_load_lds_dwordx4 v13, s[30:31] offset:3072\n"
    "  s_mov_b32 m0, 65536\n"
    "  s_nop 0\n"
    "  global_load_lds_dwordx4 v14, s[30:31] offset:0\n"
    "  global_load_lds_dwordx4 v15, s[30:31] offset:1024\n"
    "  global_load_lds_dwordx4 v16, s[30:31] offset:2048\n"
    "  global_load_lds_dwordx4 v17, s[30:31] offset:3072\n"
    "  s_mov_b32 m0, 69632\n"
    "  s_nop 0\n"
    "  global_load_lds_dwordx4 v18, s[30:31] offset:0\n"
    "  global_load_lds_dwordx4 v19, s[30:31] offset:1024\n"
    "  v_mov_b32 v38, 28\n"
    "  s_mov_b32 m0, 156160\n"
    "  s_nop 0\n"
    "  global_load_lds_dword v38, s[52:53]\n"
    "  s_add_u32 s44, s44, 1048576\n"
    "  s_addc_u32 s45, s45, 0\n"
    "  s_mov_b32 m0, 96256\n"
    "  s_nop 0\n"
    "  global_load_lds_dwordx4 v30, s[44:45] offset:0\n"
    "  global_load_lds_dwordx4 v31, s[44:45] offset:1024\n"
    "  global_load_lds_dwordx4 v32, s[44:45] offset:2048\n"
    "  global_load_lds_dwordx4 v33, s[44:45] offset:3072\n"
    "  s_mov_b32 m0, 100352\n"
    "  s_nop 0\n"
    "  global_load_lds_dwordx4 v34, s[44:45] offset:0\n"
    "  global_load_lds_dwordx4 v35, s[44:45] offset:1024\n"
    "  global_load_lds_dwordx4 v36, s[44:45] offset:2048\n"
    "  global_load_lds_dwordx4 v37, s[44:45] offset:3072\n"
    "  v_mov_b32 v38, 36\n"
    "  s_mov_b32 m0, 156416\n"
    "  s_nop 0\n"
    "  global_load_lds_dword v38, s[52:53]\n"
    "  s_add_u32 s44, s44, 1048576\n"
    "  s_addc_u32 s45, s45, 0\n"
    "  s_mov_b32 m0, 104448\n"
    "  s_nop 0\n"
    "  global_load_lds_dwordx4 v30, s[44:45] offset:0\n"
    "  global_load_lds_dwordx4 v31, s[44:45] offset:1024\n"
    "  global_load_lds_dwordx4 v32, s[44:45] offset:2048\n"
    "  global_load_lds_dwordx4 v33, s[44:45] offset:3072\n"
    "  s_mov_b32 m0, 108544\n"
    "  s_nop 0\n"
    "  global_load_lds_dwordx4 v34, s[44:45] offset:0\n"
    "  global_load_lds_dwordx4 v35, s[44:45] offset:1024\n"
    "  global_load_lds_dwordx4 v36, s[44:45] offset:2048\n"
    "  global_load_lds_dwordx4 v37, s[44:45] offset:3072\n"
    "  v_mov_b32 v38, 44\n"
    "  s_mov_b32 m0, 156416\n"
    "  s_nop 0\n"
    "  global_load_lds_dword v38, s[52:53]\n"
    "  s_add_u32 s44, s44, 1048576\n"
    "  s_addc_u32 s45, s45, 0\n"
    "  s_mov_b32 m0, 112640\n"
    "  s_nop 0\n"
    "  global_load_lds_dwordx4 v30, s[44:45] offset:0\n"
    "  global_load_lds_dwordx4 v31, s[44:45] offset:1024\n"
    "  global_load_lds_dwordx4 v32, s[44:45] offset:2048\n"
    "  global_load_lds_dwordx4 v33, s[44:45] offset:3072\n"
    "  s_mov_b32 m0, 116736\n"
    "  s_nop 0\n"
    "  global_load_lds_dwordx4 v34, s[44:45] offset:0\n"
    "  global_load_lds_dwordx4 v35, s[44:45] offset:1024\n"
    "  global_load_lds_dwordx4 v36, s[44:45] offset:2048\n"
    "  global_load_lds_dwordx4 v37, s[44:45] offset:3072\n"
    "  v_mov_b32 v38, 52\n"
    "  s_mov_b32 m0, 156416\n"
    "  s_nop 0\n"
    "  global_load_lds_dword v38, s[52:53]\n"
    "  s_lshl_b32 s23, s21, 8\n"
    "  s_add_u32 s23, s23, 153600\n"
    "  v_lshl_add_u32 v49, v3, 2, s23\n"
    "  v_and_b32 v48, 3, v3\n"
    "  v_mul_u32_u24 v48, 0x104, v48\n"
    "  v_add_u32 v48, 153600, v48\n"
    "  s_mul_i32 s23, s21, 160\n"
    "  s_add_u32 s23, s23, 3072\n"
    "  v_add_u32 v26, 0, v3\n"
    "  v_mul_u32_u24 v27, 6554, v26\n"
    "  v_lshrrev_b32 v27, 16, v27\n"
    "  v_lshlrev_b32 v26, 4, v26\n"
    "  v_mul_u32_u24 v27, 480, v27\n"
    "  v_add3_u32 v20, v26, v27, s23\n"
    "  v_add_u32 v26, 64, v3\n"
    "  v_mul_u32_u24 v27, 6554, v26\n"
    "  v_lshrrev_b32 v27, 16, v27\n"
    "  v_lshlrev_b32 v26, 4, v26\n"
    "  v_mul_u32_u24 v27, 480, v27\n"
    "  v_add3_u32 v21, v26, v27, s23\n"
    "  v_subrev_u32 v21, 1024, v21\n"
    "  v_add_u32 v26, 128, v3\n"
    "  v_mul_u32_u24 v27, 6554, v26\n"
    "  v_lshrrev_b32 v27, 16, v27\n"
    "  v_lshlrev_b32 v26, 4, v26\n"
    "  v_mul_u32_u24 v27, 480, v27\n"
    "  v_add3_u32 v22, v26, v27, s23\n"
    "  v_subrev_u32 v22, 2048, v22\n"
    "  v_add_u32 v26, 192, v3\n"
    "  v_mul_u32_u24 v27, 6554, v26\n"
    "  v_lshrrev_b32 v27, 16, v27\n"
    "  v_lshlrev_b32 v26, 4, v26\n"
    "  v_mul_u32_u24 v27, 480, v27\n"
    "  v_add3_u32 v23, v26, v27, s23\n"
    "  v_subrev_u32 v23, 3072, v23\n"
    "  v_add_u32 v26, 256, v3\n"
    "  v_mul_u32_u24 v27, 6554, v26\n"
    "  v_lshrrev_b32 v27, 16, v27\n"
    "  v_lshlrev_b32 v26, 4, v26\n"
    "  v_mul_u32_u24 v27, 480, v27\n"
    "  v_add3_u32 v24, v26, v27, s23\n"
    "  v_lshrrev_b32 v26, 2, v3\n"
    "  v_mul_u32_u24 v26, 160, v26\n"
    "  v_and_b32 v27, 3, v3\n"
    "  v_lshl_add_u32 v25, v27, 3, v26\n"
    "  v_add_u32 v29, 61440, v25\n"
    "  v_add_u32 v9, 137216, v25\n"
    "  s_sub_u32 s16, s16, 3072\n"
    "  s_subb_u32 s17, s17, 0\n"
    ".Ledm_pl1:\n"
    "  s_sub_u32 s39, s39, 1\n"
    "  s_cbranch_scc1 .Ledm_abort\n"
    "  ds_read_b32 v55, v48 offset:0\n"
    "  s_waitcnt lgkmcnt(0)\n"
    "  v_cmp_gt_u32_e32 vcc, 2, v55\n"
    "  s_cbranch_vccnz .Ledm_pl1\n"
    "  s_add_u32 s30, s30, 10240\n"
    "  s_addc_u32 s31, s31, 0\n"
    "  s_mov_b32 m0, 0\n"
    "  s_nop 0\n"
    "  global_load_lds_dwordx4 v10, s[30:31] offset:0\n"
    "  global_load_lds_dwordx4 v11, s[30:31] offset:1024\n"
    "  global_load_lds_dwordx4 v12, s[30:31] offset:2048\n"
    "  global_load_lds_dwordx4 v13, s[30:31] offset:3072\n"
    "  s_mov_b32 m0, 4096\n"
    "  s_nop 0\n"
    "  global_load_lds_dwordx4 v14, s[30:31] offset:0\n"
    "  global_load_lds_dwordx4 v15, s[30:31] offset:1024\n"
    "  global_load_lds_dwordx4 v16, s[30:31] offset:2048\n"
    "  global_load_lds_dwordx4 v17, s[30:31] offset:3072\n"
    "  s_mov_b32 m0, 8192\n"
    "  s_nop 0\n"
    "  global_load_lds_dwordx4 v18, s[30:31] offset:0\n"
    "  global_load_lds_dwordx4 v19, s[30:31] offset:1024\n"
    "  v_mov_b32 v38, 32\n"
    "  s_mov_b32 m0, 156160\n"
    "  s_nop 0\n"
    "  global_load_lds_dword v38, s[52:53]\n"
    "  s_add_u32 s30, s30, 10240\n"
    "  s_addc_u32 s31, s31, 0\n"
    "  s_mov_b32 m0, 10240\n"
    "  s_nop 0\n"
    "  global_load_lds_dwordx4 v10, s[30:31] offset:0\n"
    "  global_load_lds_dwordx4 v11, s[30:31] offset:1024\n"
    "  global_load_lds_dwordx4 v12, s[30:31] offset:2048\n"
    "  global_load_lds_dwordx4 v13, s[30:31] offset:3072\n"
    "  s_mov_b32 m0, 14336\n"
    "  s_nop 0\n"
    "  global_load_lds_dwordx4 v14, s[30:31] offset:0\n"
    "  global_load_lds_dwordx4 v15, s[30:31] offset:1024\n"
    "  global_load_lds_dwordx4 v16, s[30:31] offset:2048\n"
    "  global_load_lds_dwordx4 v17, s[30:31] offset:3072\n"
    "  s_mov_b32 m0, 18432\n"
    "  s_nop 0\n"
    "  global_load_lds_dwordx4 v18, s[30:31] offset:0\n"
    "  global_load_lds_dwordx4 v19, s[30:31] offset:1024\n"
    "  v_mov_b32 v38, 36\n"
    "  s_mov_b32 m0, 156160\n"
    "  s_nop 0\n"
    "  global_load_lds_dword v38, s[52:53]\n"
    ".Ledm_pl2:\n"
    "  s_sub_u32 s39, s39, 1\n"
    "  s_cbranch_scc1 .Ledm_abort\n"
    "  ds_read_b32 v55, v48 offset:0\n"
    "  s_waitcnt lgkmcnt(0)\n"
    "  v_cmp_gt_u32_e32 vcc, 4, v55\n"
    "  s_cbranch_vccnz .Ledm_pl2\n"
    "  s_add_u32 s30, s30, 10240\n"
    "  s_addc_u32 s31, s31, 0\n"
    "  s_mov_b32 m0, 20480\n"
    "  s_nop 0\n"
    "  global_load_lds_dwordx4 v10, s[30:31] offset:0\n"
    "  global_load_lds_dwordx4 v11, s[30:31] offset:1024\n"
    "  global_load_lds_dwordx4 v12, s[30:31] offset:2048\n"
    "  global_load_lds_dwordx4 v13, s[30:31] offset:3072\n"
    "  s_mov_b32 m0, 24576\n"
    "  s_nop 0\n"
    "  global_load_lds_dwordx4 v14, s[30:31] offset:0\n"
    "  global_load_lds_dwordx4 v15, s[30:31] offset:1024\n"
    "  global_load_lds_dwordx4 v16, s[30:31] offset:2048\n"
    "  global_load_lds_dwordx4 v17, s[30:31] offset:3072\n"
    "  s_mov_b32 m0, 28672\n"
    "  s_nop 0\n"
    "  global_load_lds_dwordx4 v18, s[30:31] offset:0\n"
    "  global_load_lds_dwordx4 v19, s[30:31] offset:1024\n"
    "  v_mov_b32 v38, 40\n"
    "  s_mov_b32 m0, 156160\n"
    "  s_nop 0\n"
    "  global_load_lds_dword v38, s[52:53]\n"
    "  s_add_u32 s30, s30, 10240\n"
    "  s_addc_u32 s31, s31, 0\n"
    "  s_mov_b32 m0, 30720\n"
    "  s_nop 0\n"
    "  global_load_lds_dwordx4 v10, s[30:31] offset:0\n"
    "  global_load_lds_dwordx4 v11, s[30:31] offset:1024\n"
    "  global_load_lds_dwordx4 v12, s[30:31] offset:2048\n"
    "  global_load_lds_dwordx4 v13, s[30:31] offset:3072\n"
    "  s_mov_b32 m0, 34816\n"
    "  s_nop 0\n"
    "  global_load_lds_dwordx4 v14, s[30:31] offset:0\n"
    "  global_load_lds_dwordx4 v15, s[30:31] offset:1024\n"
    "  global_load_lds_dwordx4 v16, s[30:31] offset:2048\n"
    "  global_load_lds_dwordx4 v17, s[30:31] offset:3072\n"
    "  s_mov_b32 m0, 38912\n"
    "  s_nop 0\n"
    "  global_load_lds_dwordx4 v18, s[30:31] offset:0\n"
    "  global_load_lds_dwordx4 v19, s[30:31] offset:1024\n"
    "  v_mov_b32 v38, 44\n"
    "  s_mov_b32 m0, 156160\n"
    "  s_nop 0\n"
    "  global_load_lds_dword v38, s[52:53]\n"
    ".Ledm_pl3:\n"
    "  s_sub_u32 s39, s39, 1\n"
    "  s_cbranch_scc1 .Ledm_abort\n"
    "  ds_read_b32 v55, v48 offset:0\n"
    "  s_waitcnt lgkmcnt(0)\n"
    "  v_cmp_gt_u32_e32 vcc, 6, v55\n"
    "  s_cbranch_vccnz .Ledm_pl3\n"
    "  s_add_u32 s30, s30, 10240\n"
    "  s_addc_u32 s31, s31, 0\n"
    "  s_mov_b32 m0, 40960\n"
    "  s_nop 0\n"
    "  global_load_lds_dwordx4 v10, s[30:31] offset:0\n"
    "  global_load_lds_dwordx4 v11, s[30:31] offset:1024\n"
    "  global_load_lds_dwordx4 v12, s[30:31] offset:2048\n"
    "  global_load_lds_dwordx4 v13, s[30:31] offset:3072\n"
    "  s_mov_b32 m0, 45056\n"
    "  s_nop 0\n"
    "  global_load_lds_dwordx4 v14, s[30:31] offset:0\n"
    "  global_load_lds_dwordx4 v15, s[30:31] offset:1024\n"
    "  global_load_lds_dwordx4 v16, s[30:31] offset:2048\n"
    "  global_load_lds_dwordx4 v17, s[30:31] offset:3072\n"
    "  s_mov_b32 m0, 49152\n"
    "  s_nop 0\n"
    "  global_load_lds_dwordx4 v18, s[30:31] offset:0\n"
    "  global_load_lds_dwordx4 v19, s[30:31] offset:1024\n"
    "  v_mov_b32 v38, 48\n"
    "  s_mov_b32 m0, 156160\n"
    "  s_nop 0\n"
    "  global_load_lds_dword v38, s[52:53]\n"
    "  s_add_u32 s44, s44, 1048576\n"
    "  s_addc_u32 s45, s45, 0\n"
    "  s_mov_b32 m0, 120832\n"
    "  s_nop 0\n"
    "  global_load_lds_dwordx4 v30, s[44:45] offset:0\n"
    "  global_load_lds_dwordx4 v31, s[44:45] offset:1024\n"
    "  global_load_lds_dwordx4 v32, s[44:45] offset:2048\n"
    "  global_load_lds_dwordx4 v33, s[44:45] offset:3072\n"
    "  s_mov_b32 m0, 124928\n"
    "  s_nop 0\n"
    "  global_load_lds_dwordx4 v34, s[44:45] offset:0\n"
    "  global_load_lds_dwordx4 v35, s[44:45] offset:1024\n"
    "  global_load_lds_dwordx4 v36, s[44:45] offset:2048\n"
    "  global_load_lds_dwordx4 v37, s[44:45] offset:3072\n"
    "  v_mov_b32 v38, 60\n"
    "  s_mov_b32 m0, 156416\n"
    "  s_nop 0\n"
    "  global_load_lds_dword v38, s[52:53]\n"
    "  s_add_u32 s44, s44, 1048576\n"
    "  s_addc_u32 s45, s45, 0\n"
    "  s_mov_b32 m0, 129024\n"
    "  s_nop 0\n"
    "  global_load_lds_dwordx4 v30, s[44:45] offset:0\n"
    "  global_load_lds_dwordx4 v31, s[44:45] offset:1024\n"
    "  global_load_lds_dwordx4 v32, s[44:45] offset:2048\n"
    "  global_load_lds_dwordx4 v33, s[44:45] offset:3072\n"
    "  s_mov_b32 m0, 133120\n"
    "  s_nop 0\n"
    "  global_load_lds_dwordx4 v34, s[44:45] offset:0\n"
    "  global_load_lds_dwordx4 v35, s[44:45] offset:1024\n"
    "  global_load_lds_dwordx4 v36, s[44:45] offset:2048\n"
    "  global_load_lds_dwordx4 v37, s[44:45] offset:3072\n"
    "  v_mov_b32 v38, 68\n"
    "  s_mov_b32 m0, 156416\n"
    "  s_nop 0\n"
    "  global_load_lds_dword v38, s[52:53]\n"
    "  s_add_u32 s30, s30, 10240\n"
    "  s_addc_u32 s31, s31, 0\n"
    "  s_mov_b32 m0, 51200\n"
    "  s_nop 0\n"
    "  global_load_lds_dwordx4 v10, s[30:31] offset:0\n"
    "  global_load_lds_dwordx4 v11, s[30:31] offset:1024\n"
    "  global_load_lds_dwordx4 v12, s[30:31] offset:2048\n"
    "  global_load_lds_dwordx4 v13, s[30:31] offset:3072\n"
    "  s_mov_b32 m0, 55296\n"
    "  s_nop 0\n"
    "  global_load_lds_dwordx4 v14, s[30:31] offset:0\n"
    "  global_load_lds_dwordx4 v15, s[30:31] offset:1024\n"
    "  global_load_lds_dwordx4 v16, s[30:31] offset:2048\n"
    "  global_load_lds_dwordx4 v17, s[30:31] offset:3072\n"
    "  s_mov_b32 m0, 59392\n"
    "  s_nop 0\n"
    "  global_load_lds_dwordx4 v18, s[30:31] offset:0\n"
    "  global_load_lds_dwordx4 v19, s[30:31] offset:1024\n"
    "  v_mov_b32 v38, 52\n"
    "  s_mov_b32 m0, 156160\n"
    "  s_nop 0\n"
    "  global_load_lds_dword v38, s[52:53]\n"
    ".Ledm_pl4:\n"
    "  s_sub_u32 s39, s39, 1\n"
    "  s_cbranch_scc1 .Ledm_abort\n"
    "  ds_read_b32 v55, v48 offset:0\n"
    "  s_waitcnt lgkmcnt(0)\n"
    "  v_cmp_gt_u32_e32 vcc, 8, v55\n"
    "  s_cbranch_vccnz .Ledm_pl4\n"
    "  s_add_u32 s30, s30, 10240\n"
    "  s_addc_u32 s31, s31, 0\n"
    "  s_mov_b32 m0, 61440\n"
    "  s_nop 0\n"
    "  global_load_lds_dwordx4 v10, s[30:31] offset:0\n"
    "  global_load_lds_dwordx4 v11, s[30:31] offset:1024\n"
    "  global_load_lds_dwordx4 v12, s[30:31] offset:2048\n"
    "  global_load_lds_dwordx4 v13, s[30:31] offset:3072\n"
    "  s_mov_b32 m0, 65536\n"
    "  s_nop 0\n"
    "  global_load_lds_dwordx4 v14, s[30:31] offset:0\n"
    "  global_load_lds_dwordx4 v15, s[30:31] offset:1024\n"
    "  global_load_lds_dwordx4 v16, s[30:31] offset:2048\n"
    "  global_load_lds_dwordx4 v17, s[30:31] offset:3072\n"
    "  s_mov_b32 m0, 69632\n"
    "  s_nop 0\n"
    "  global_load_lds_dwordx4 v18, s[30:31] offset:0\n"
    "  global_load_lds_dwordx4 v19, s[30:31] offset:1024\n"
    "  v_mov_b32 v38, 56\n"
    "  s_mov_b32 m0, 156160\n"
    "  s_nop 0\n"
    "  global_load_lds_dword v38, s[52:53]\n"
    "  s_add_u32 s30, s30, 10240\n"
    "  s_addc_u32 s31, s31, 0\n"
    "  s_mov_b32 m0, 0\n"
    "  s_nop 0\n"
    "  global_load_lds_dwordx4 v10, s[30:31] offset:0\n"
    "  global_load_lds_dwordx4 v11, s[30:31] offset:1024\n"
    "  global_load_lds_dwordx4 v12, s[30:31] offset:2048\n"
    "  global_load_lds_dwordx4 v13, s[30:31] offset:3072\n"
    "  s_mov_b32 m0, 4096\n"
    "  s_nop 0\n"
    "  global_load_lds_dwordx4 v14, s[30:31] offset:0\n"
    "  global_load_lds_dwordx4 v15, s[30:31] offset:1024\n"
    "  global_load_lds_dwordx4 v16, s[30:31] offset:2048\n"
    "  global_load_lds_dwordx4 v17, s[30:31] offset:3072\n"
    "  s_mov_b32 m0, 8192\n"
    "  s_nop 0\n"
    "  global_load_lds_dwordx4 v18, s[30:31] offset:0\n"
    "  global_load_lds_dwordx4 v19, s[30:31] offset:1024\n"
    "  v_mov_b32 v38, 60\n"
    "  s_mov_b32 m0, 156160\n"
    "  s_nop 0\n"
    "  global_load_lds_dword v38, s[52:53]\n"
    ".Ledm_pl5:\n"
    "  s_sub_u32 s39, s39, 1\n"
    "  s_cbranch_scc1 .Ledm_abort\n"
    "  ds_read_b32 v55, v48 offset:0\n"
    "  s_waitcnt lgkmcnt(0)\n"
    "  v_cmp_gt_u32_e32 vcc, 10, v55\n"
    "  s_cbranch_vccnz .Ledm_pl5\n"
    "  s_add_u32 s30, s30, 10240\n"
    "  s_addc_u32 s31, s31, 0\n"
    "  s_mov_b32 m0, 10240\n"
    "  s_nop 0\n"
    "  global_load_lds_dwordx4 v10, s[30:31] offset:0\n"
    "  global_load_lds_dwordx4 v11, s[30:31] offset:1024\n"
    "  global_load_lds_dwordx4 v12, s[30:31] offset:2048\n"
    "  global_load_lds_dwordx4 v13, s[30:31] offset:3072\n"
    "  s_mov_b32 m0, 14336\n"
    "  s_nop 0\n"
    "  global_load_lds_dwordx4 v14, s[30:31] offset:0\n"
    "  global_load_lds_dwordx4 v15, s[30:31] offset:1024\n"
    "  global_load_lds_dwordx4 v16, s[30:31] offset:2048\n"
    "  global_load_lds_dwordx4 v17, s[30:31] offset:3072\n"
    "  s_mov_b32 m0, 18432\n"
    "  s_nop 0\n"
    "  global_load_lds_dwordx4 v18, s[30:31] offset:0\n"
    "  global_load_lds_dwordx4 v19, s[30:31] offset:1024\n"
    "  v_mov_b32 v38, 64\n"
    "  s_mov_b32 m0, 156160\n"
    "  s_nop 0\n"
    "  global_load_lds_dword v38, s[52:53]\n"
    "  s_add_u32 s44, s44, 1048576\n"
    "  s_addc_u32 s45, s45, 0\n"
    "  s_mov_b32 m0, 137216\n"
    "  s_nop 0\n"
    "  global_load_lds_dwordx4 v30, s[44:45] offset:0\n"
    "  global_load_lds_dwordx4 v31, s[44:45] offset:1024\n"
    "  global_load_lds_dwordx4 v32, s[44:45] offset:2048\n"
    "  global_load_lds_dwordx4 v33, s[44:45] offset:3072\n"
    "  s_mov_b32 m0, 141312\n"
    "  s_nop 0\n"
    "  global_load_lds_dwordx4 v34, s[44:45] offset:0\n"
    "  global_load_lds_dwordx4 v35, s[44:45] offset:1024\n"
    "  global_load_lds_dwordx4 v36, s[44:45] offset:2048\n"
    "  global_load_lds_dwordx4 v37, s[44:45] offset:3072\n"
    "  v_mov_b32 v38, 76\n"
    "  s_mov_b32 m0, 156416\n"
    "  s_nop 0\n"
    "  global_load_lds_dword v38, s[52:53]\n"
    "  s_add_u32 s44, s44, 1048576\n"
    "  s_addc_u32 s45, s45, 0\n"
    "  s_mov_b32 m0, 145408\n"
    "  s_nop 0\n"
    "  global_load_lds_dwordx4 v30, s[44:45] offset:0\n"
    "  global_load_lds_dwordx4 v31, s[44:45] offset:1024\n"
    "  global_load_lds_dwordx4 v32, s[44:45] offset:2048\n"
    "  global_load_lds_dwordx4 v33, s[44:45] offset:3072\n"
    "  s_mov_b32 m0, 149504\n"
    "  s_nop 0\n"
    "  global_load_lds_dwordx4 v34, s[44:45] offset:0\n"
    "  global_load_lds_dwordx4 v35, s[44:45] offset:1024\n"
    "  global_load_lds_dwordx4 v36, s[44:45] offset:2048\n"
    "  global_load_lds_dwordx4 v37, s[44:45] offset:3072\n"
    "  v_mov_b32 v38, 84\n"
    "  s_mov_b32 m0, 156416\n"
    "  s_nop 0\n"
    "  global_load_lds_dword v38, s[52:53]\n"
    "  s_add_u32 s30, s30, 10240\n"
    "  s_addc_u32 s31, s31, 0\n"
    "  s_mov_b32 m0, 20480\n"
    "  s_nop 0\n"
    "  global_load_lds_dwordx4 v10, s[30:31] offset:0\n"
    "  global_load_lds_dwordx4 v11, s[30:31] offset:1024\n"
    "  global_load_lds_dwordx4 v12, s[30:31] offset:2048\n"
    "  global_load_lds_dwordx4 v13, s[30:31] offset:3072\n"
    "  s_mov_b32 m0, 24576\n"
    "  s_nop 0\n"
    "  global_load_lds_dwordx4 v14, s[30:31] offset:0\n"
    "  global_load_lds_dwordx4 v15, s[30:31] offset:1024\n"
    "  global_load_lds_dwordx4 v16, s[30:31] offset:2048\n"
    "  global_load_lds_dwordx4 v17, s[30:31] offset:3072\n"
    "  s_mov_b32 m0, 28672\n"
    "  s_nop 0\n"
    "  global_load_lds_dwordx4 v18, s[30:31] offset:0\n"
    "  global_load_lds_dwordx4 v19, s[30:31] offset:1024\n"
    "  v_mov_b32 v38, 68\n"
    "  s_mov_b32 m0, 156160\n"
    "  s_nop 0\n"
    "  global_load_lds_dword v38, s[52:53]\n"
    ".Ledm_pl6:\n"
    "  s_sub_u32 s39, s39, 1\n"
    "  s_cbranch_scc1 .Ledm_abort\n"
    "  ds_read_b32 v55, v48 offset:0\n"
    "  s_waitcnt lgkmcnt(0)\n"
    "  v_cmp_gt_u32_e32 vcc, 12, v55\n"
    "  s_cbranch_vccnz .Ledm_pl6\n"
    "  s_add_u32 s30, s30, 10240\n"
    "  s_addc_u32 s31, s31, 0\n"
    "  s_mov_b32 m0, 30720\n"
    "  s_nop 0\n"
    "  global_load_lds_dwordx4 v10, s[30:31] offset:0\n"
    "  global_load_lds_dwordx4 v11, s[30:31] offset:1024\n"
    "  global_load_lds_dwordx4 v12, s[30:31] offset:2048\n"
    "  global_load_lds_dwordx4 v13, s[30:31] offset:3072\n"
    "  s_mov_b32 m0, 34816\n"
    "  s_nop 0\n"
    "  global_load_lds_dwordx4 v14, s[30:31] offset:0\n"
    "  global_load_lds_dwordx4 v15, s[30:31] offset:1024\n"
    "  global_load_lds_dwordx4 v16, s[30:31] offset:2048\n"
    "  global_load_lds_dwordx4 v17, s[30:31] offset:3072\n"
    "  s_mov_b32 m0, 38912\n"
    "  s_nop 0\n"
    "  global_load_lds_dwordx4 v18, s[30:31] offset:0\n"
    "  global_load_lds_dwordx4 v19, s[30:31] offset:1024\n"
    "  v_mov_b32 v38, 72\n"
    "  s_mov_b32 m0, 156160\n"
    "  s_nop 0\n"
    "  global_load_lds_dword v38, s[52:53]\n"
    "  s_add_u32 s30, s30, 10240\n"
    "  s_addc_u32 s31, s31, 0\n"
    "  s_mov_b32 m0, 40960\n"
    "  s_nop 0\n"
    "  global_load_lds_dwordx4 v10, s[30:31] offset:0\n"
    "  global_load_lds_dwordx4 v11, s[30:31] offset:1024\n"
    "  global_load_lds_dwordx4 v12, s[30:31] offset:2048\n"
    "  global_load_lds_dwordx4 v13, s[30:31] offset:3072\n"
    "  s_mov_b32 m0, 45056\n"
    "  s_nop 0\n"
    "  global_load_lds_dwordx4 v14, s[30:31] offset:0\n"
    "  global_load_lds_dwordx4 v15, s[30:31] offset:1024\n"
    "  global_load_lds_dwordx4 v16, s[30:31] offset:2048\n"
    "  global_load_lds_dwordx4 v17, s[30:31] offset:3072\n"
    "  s_mov_b32 m0, 49152\n"
    "  s_nop 0\n"
    "  global_load_lds_dwordx4 v18, s[30:31] offset:0\n"
    "  global_load_lds_dwordx4 v19, s[30:31] offset:1024\n"
    "  v_mov_b32 v38, 76\n"
    "  s_mov_b32 m0, 156160\n"
    "  s_nop 0\n"
    "  global_load_lds_dword v38, s[52:53]\n"
    ".Ledm_pl7:\n"
    "  s_sub_u32 s39, s39, 1\n"
    "  s_cbranch_scc1 .Ledm_abort\n"
    "  ds_read_b32 v55, v48 offset:0\n"
    "  s_waitcnt lgkmcnt(0)\n"
    "  v_cmp_gt_u32_e32 vcc, 14, v55\n"
    "  s_cbranch_vccnz .Ledm_pl7\n"
    "  s_add_u32 s30, s30, 10240\n"
    "  s_addc_u32 s31, s31, 0\n"
    "  s_mov_b32 m0, 51200\n"
    "  s_nop 0\n"
    "  global_load_lds_dwordx4 v10, s[30:31] offset:0\n"
    "  global_load_lds_dwordx4 v11, s[30:31] offset:1024\n"
    "  global_load_lds_dwordx4 v12, s[30:31] offset:2048\n"
    "  global_load_lds_dwordx4 v13, s[30:31] offset:3072\n"
    "  s_mov_b32 m0, 55296\n"
    "  s_nop 0\n"
    "  global_load_lds_dwordx4 v14, s[30:31] offset:0\n"
    "  global_load_lds_dwordx4 v15, s[30:31] offset:1024\n"
    "  global_load_lds_dwordx4 v16, s[30:31] offset:2048\n"
    "  global_load_lds_dwordx4 v17, s[30:31] offset:3072\n"
    "  s_mov_b32 m0, 59392\n"
    "  s_nop 0\n"
    "  global_load_lds_dwordx4 v18, s[30:31] offset:0\n"
    "  global_load_lds_dwordx4 v19, s[30:31] offset:1024\n"
    "  v_mov_b32 v38, 80\n"
    "  s_mov_b32 m0, 156160\n"
    "  s_nop 0\n"
    "  global_load_lds_dword v38, s[52:53]\n"
    "  v_lshrrev_b32 v27, 2, v3\n"
    "  v_lshlrev_b32 v26, 7, v27\n"
    "  v_bfe_u32 v28, v3, 3, 2\n"
    "  v_lshl_or_b32 v26, v28, 5, v26\n"
    "  v_and_b32 v28, 3, v3\n"
    "  v_lshl_or_b32 v26, v28, 3, v26\n"
    "  s_mul_i32 s26, s21, 20480\n"
    "  s_add_u32 s26, s26, 71680\n"
    "  v_add_u32 v53, s26, v26\n"
    "  v_bfe_u32 v27, v3, 2, 2\n"
    "  v_mul_u32_u24 v46, 0x120, v27\n"
    "  v_lshrrev_b32 v28, 5, v3\n"
    "  v_lshl_or_b32 v46, v28, 7, v46\n"
    "  v_bfe_u32 v28, v3, 4, 1\n"
    "  v_lshl_or_b32 v46, v28, 10, v46\n"
    "  v_and_b32 v28, 3, v3\n"
    "  v_lshl_or_b32 v46, v28, 3, v46\n"
    "  v_add_u32 v46, s26, v46\n"
    "  v_lshrrev_b32 v27, 2, v3\n"
    "  v_lshlrev_b32 v28, 4, v28\n"
    "  s_mul_i32 s23, s21, 80\n"
    "  v_add_u32 v54, s23, v27\n"
    "  v_lshl_or_b32 v54, v54, 15, v28\n"
    "  v_and_b32 v27, 3, v27\n"
    "  v_lshrrev_b32 v28, 4, v3\n"
    "  v_mul_u32_u24 v47, 0x120, v28\n"
    "  v_lshl_or_b32 v47, v27, 3, v47\n"
    "  v_and_b32 v28, 1, v3\n"
    "  v_lshl_or_b32 v47, v28, 7, v47\n"
    "  v_bfe_u32 v28, v3, 1, 1\n"
    "  v_lshlrev_b32 v28, 5, v28\n"
    "  v_xor_b32 v47, v28, v47\n"
    "  v_add_u32 v47, s26, v47\n"
    "  v_lshlrev_b32 v52, 4, v3\n"
    "  s_add_u32 s26, s24, 0\n"
    "  s_add_u32 s40, s18, s26\n"
    "  s_addc_u32 s41, s19, 0\n"
    "  s_add_u32 s26, s24, 524288\n"
    "  s_add_u32 s42, s18, s26\n"
    "  s_addc_u32 s43, s19, 0\n"
    "  s_add_u32 s26, s24, 1048576\n"
    "  s_add_u32 s44, s18, s26\n"
    "  s_addc_u32 s45, s19, 0\n"
    "  s_add_u32 s26, s24, 1572864\n"
    "  s_add_u32 s46, s18, s26\n"
    "  s_addc_u32 s47, s19, 0\n"
    "  s_add_u32 s26, s24, 2097152\n"
    "  s_add_u32 s48, s18, s26\n"
    "  s_addc_u32 s49, s19, 0\n"
    "  s_waitcnt lgkmcnt(0)\n"
    "  s_lshl_b32 s23, s28, 4\n"
    "  s_lshr_b32 s38, s38, s23\n"
    "  s_lshl_b32 s38, s38, 16\n"
    "  v_mul_f32 v26, s38, s38\n"
    "  v_cvt_pk_bf16_f32 v26, v26, v26\n"
    "  v_lshlrev_b32 v26, 16, v26\n"
    "  v_add_f32 v26, 0x3e800000, v26\n"
    "  v_cvt_pk_bf16_f32 v26, v26, v26\n"
    "  v_lshlrev_b32 v26, 16, v26\n"
    "  v_rcp_f32 v26, v26\n"
    "  s_nop 0\n"
    "  v_mul_f32 v26, 0x3e800000, v26\n"
    "  v_cvt_pk_bf16_f32 v26, v26, v26\n"
    "  v_and_b32 v26, 0xffff, v26\n"
    "  v_and_b32 v27, 1, v3\n"
    "  v_lshlrev_b32 v27, 4, v27\n"
    "  v_lshlrev_b32 v26, v27, v26\n"
    "  v_bfe_u32 v27, v3, 1, 3\n"
    "  v_lshrrev_b32 v28, 4, v3\n"
    "  v_lshlrev_b32 v28, 1, v28\n"
    "  v_cmp_eq_u32_e32 vcc, v27, v28\n"
    "  s_nop 1\n"
    "  v_cndmask_b32_e32 v50, 0, v26, vcc\n"
    "  v_add_u32 v28, 1, v28\n"
    "  v_cmp_eq_u32_e32 vcc, v27, v28\n"
    "  s_nop 1\n"
    "  v_cndmask_b32_e32 v51, 0, v26, vcc\n"
    "  s_add_u32 s54, s16, 0\n"
    "  s_addc_u32 s55, s17, 0\n"
    "  s_mov_b32 m0, 61440\n"
    "  s_nop 0\n"
    "  global_load_lds_dwordx4 v20, s[54:55] offset:0\n"
    "  global_load_lds_dwordx4 v21, s[54:55] offset:1024\n"
    "  global_load_lds_dwordx4 v22, s[54:55] offset:2048\n"
    "  global_load_lds_dwordx4 v23, s[54:55] offset:3072\n"
    "  s_mov_b32 m0, 65536\n"
    "  s_nop 0\n"
    "  global_load_lds_dwordx4 v24, s[54:55] offset:0\n"
    "  s_add_u32 s54, s16, 20480\n"
    "  s_addc_u32 s55, s17, 0\n"
    "  s_mov_b32 m0, 66560\n"
    "  s_nop 0\n"
    "  global_load_lds_dwordx4 v20, s[54:55] offset:0\n"
    "  global_load_lds_dwordx4 v21, s[54:55] offset:1024\n"
    "  global_load_lds_dwordx4 v22, s[54:55] offset:2048\n"
    "  global_load_lds_dwordx4 v23, s[54:55] offset:3072\n"
    "  s_mov_b32 m0, 70656\n"
    "  s_nop 0\n"
    "  global_load_lds_dwordx4 v24, s[54:55] offset:0\n"
    "  s_waitcnt vmcnt(5)\n"
    "  ds_read_b64_tr_b16 v[56:57], v29 offset:0\n"
    "  ds_read_b64_tr_b16 v[58:59], v29 offset:2560\n"
    "  ds_read_b64_tr_b16 v[60:61], v29 offset:32\n"
    "  ds_read_b64_tr_b16 v[62:63], v29 offset:2592\n"
    "  ds_read_b64_tr_b16 v[64:65], v29 offset:64\n"
    "  ds_read_b64_tr_b16 v[66:67], v29 offset:2624\n"
    "  ds_read_b64_tr_b16 v[68:69], v29 offset:96\n"
    "  ds_read_b64_tr_b16 v[70:71], v29 offset:2656\n"
    "  ds_read_b64_tr_b16 v[72:73], v29 offset:128\n"
    "  ds_read_b64_tr_b16 v[74:75], v29 offset:2688\n"
    "  s_waitcnt lgkmcnt(0)\n"
    "  s_add_u32 s54, s16, 40960\n"
    "  s_addc_u32 s55, s17, 0\n"
    "  s_mov_b32 m0, 61440\n"
    "  s_nop 0\n"
    "  global_load_lds_dwordx4 v20, s[54:55] offset:0\n"
    "  global_load_lds_dwordx4 v21, s[54:55] offset:1024\n"
    "  global_load_lds_dwordx4 v22, s[54:55] offset:2048\n"
    "  global_load_lds_dwordx4 v23, s[54:55] offset:3072\n"
    "  s_mov_b32 m0, 65536\n"
    "  s_nop 0\n"
    "  global_load_lds_dwordx4 v24, s[54:55] offset:0\n"
    "  s_waitcnt vmcnt(5)\n"
    "  ds_read_b64_tr_b16 v[76:77], v29 offset:5120\n"
    "  ds_read_b64_tr_b16 v[78:79], v29 offset:7680\n"
    "  ds_read_b64_tr_b16 v[80:81], v29 offset:5152\n"
    "  ds_read_b64_tr_b16 v[82:83], v29 offset:7712\n"
    "  ds_read_b64_tr_b16 v[84:85], v29 offset:5184\n"
    "  ds_read_b64_tr_b16 v[86:87], v29 offset:7744\n"
    "  ds_read_b64_tr_b16 v[88:89], v29 offset:5216\n"
    "  ds_read_b64_tr_b16 v[90:91], v29 offset:7776\n"
    "  ds_read_b64_tr_b16 v[92:93], v29 offset:5248\n"
    "  ds_read_b64_tr_b16 v[94:95], v29 offset:7808\n"
    "  s_waitcnt lgkmcnt(0)\n"
    "  s_add_u32 s54, s16, 61440\n"
    "  s_addc_u32 s55, s17, 0\n"
    "  s_mov_b32 m0, 66560\n"
    "  s_nop 0\n"
    "  global_load_lds_dwordx4 v20, s[54:55] offset:0\n"
    "  global_load_lds_dwordx4 v21, s[54:55] offset:1024\n"
    "  global_load_lds_dwordx4 v22, s[54:55] offset:2048\n"
    "  global_load_lds_dwordx4 v23, s[54:55] offset:3072\n"
    "  s_mov_b32 m0, 70656\n"
    "  s_nop 0\n"
    "  global_load_lds_dwordx4 v24, s[54:55] offset:0\n"
    "  s_waitcnt vmcnt(5)\n"
    "  ds_read_b64_tr_b16 v[96:97], v29 offset:0\n"
    "  ds_read_b64_tr_b16 v[98:99], v29 offset:2560\n"
    "  ds_read_b64_tr_b16 v[100:101], v29 offset:32\n"
    "  ds_read_b64_tr_b16 v[102:103], v29 offset:2592\n"
    "  ds_read_b64_tr_b16 v[104:105], v29 offset:64\n"
    "  ds_read_b64_tr_b16 v[106:107], v29 offset:2624\n"
    "  ds_read_b64_tr_b16 v[108:109], v29 offset:96\n"
    "  ds_read_b64_tr_b16 v[110:111], v29 offset:2656\n"
    "  ds_read_b64_tr_b16 v[112:113], v29 offset:128\n"
    "  ds_read_b64_tr_b16 v[114:115], v29 offset:2688\n"
    "  s_waitcnt lgkmcnt(0)\n"
    "  s_add_u32 s54, s16, 81920\n"
    "  s_addc_u32 s55, s17, 0\n"
    "  s_mov_b32 m0, 61440\n"
    "  s_nop 0\n"
    "  global_load_lds_dwordx4 v20, s[54:55] offset:0\n"
    "  global_load_lds_dwordx4 v21, s[54:55] offset:1024\n"
    "  global_load_lds_dwordx4 v22, s[54:55] offset:2048\n"
    "  global_load_lds_dwordx4 v23, s[54:55] offset:3072\n"
    "  s_mov_b32 m0, 65536\n"
    "  s_nop 0\n"
    "  global_load_lds_dwordx4 v24, s[54:55] offset:0\n"
    "  s_waitcnt vmcnt(5)\n"
    "  ds_read_b64_tr_b16 v[116:117], v29 offset:5120\n"
    "  ds_read_b64_tr_b16 v[118:119], v29 offset:7680\n"
    "  ds_read_b64_tr_b16 v[120:121], v29 offset:5152\n"
    "  ds_read_b64_tr_b16 v[122:123], v29 offset:7712\n"
    "  ds_read_b64_tr_b16 v[124:125], v29 offset:5184\n"
    "  ds_read_b64_tr_b16 v[126:127], v29 offset:7744\n"
    "  ds_read_b64_tr_b16 v[128:129], v29 offset:5216\n"
    "  ds_read_b64_tr_b16 v[130:131], v29 offset:7776\n"
    "  ds_read_b64_tr_b16 v[132:133], v29 offset:5248\n"
    "  ds_read_b64_tr_b16 v[134:135], v29 offset:7808\n"
    "  s_waitcnt lgkmcnt(0)\n"
    "  s_add_u32 s54, s16, 102400\n"
    "  s_addc_u32 s55, s17, 0\n"
    "  s_mov_b32 m0, 66560\n"
    "  s_nop 0\n"
    "  global_load_lds_dwordx4 v20, s[54:55] offset:0\n"
    "  global_load_lds_dwordx4 v21, s[54:55] offset:1024\n"
    "  global_load_lds_dwordx4 v22, s[54:55] offset:2048\n"
    "  global_load_lds_dwordx4 v23, s[54:55] offset:3072\n"
    "  s_mov_b32 m0, 70656\n"
    "  s_nop 0\n"
    "  global_load_lds_dwordx4 v24, s[54:55] offset:0\n"
    ".Ledm_pl8:\n"
    "  s_sub_u32 s39, s39, 1\n"
    "  s_cbranch_scc1 .Ledm_abort\n"
    "  ds_read_b32 v55, v48 offset:0\n"
    "  s_waitcnt lgkmcnt(0)\n"
    "  v_cmp_gt_u32_e32 vcc, 18, v55\n"
    "  s_cbranch_vccnz .Ledm_pl8\n"
    "  s_add_u32 s54, s16, 122880\n"
    "  s_addc_u32 s55, s17, 0\n"
    "  s_mov_b32 m0, 20480\n"
    "  s_nop 0\n"
    "  global_load_lds_dwordx4 v20, s[54:55] offset:0\n"
    "  global_load_lds_dwordx4 v21, s[54:55] offset:1024\n"
    "  global_load_lds_dwordx4 v22, s[54:55] offset:2048\n"
    "  global_load_lds_dwordx4 v23, s[54:55] offset:3072\n"
    "  s_mov_b32 m0, 24576\n"
    "  s_nop 0\n"
    "  global_load_lds_dwordx4 v24, s[54:55] offset:0\n"
    "  s_waitcnt vmcnt(10)\n"
    "  ds_read_b64_tr_b16 v[136:137], v29 offset:0\n"
    "  ds_read_b64_tr_b16 v[138:139], v29 offset:2560\n"
    "  ds_read_b64_tr_b16 v[140:141], v29 offset:32\n"
    "  ds_read_b64_tr_b16 v[142:143], v29 offset:2592\n"
    "  ds_read_b64_tr_b16 v[144:145], v29 offset:64\n"
    "  ds_read_b64_tr_b16 v[146:147], v29 offset:2624\n"
    "  ds_read_b64_tr_b16 v[148:149], v29 offset:96\n"
    "  ds_read_b64_tr_b16 v[150:151], v29 offset:2656\n"
    "  ds_read_b64_tr_b16 v[152:153], v29 offset:128\n"
    "  ds_read_b64_tr_b16 v[154:155], v29 offset:2688\n"
    "  s_waitcnt lgkmcnt(0)\n"
    "  s_add_u32 s54, s16, 143360\n"
    "  s_addc_u32 s55, s17, 0\n"
    "  s_mov_b32 m0, 61440\n"
    "  s_nop 0\n"
    "  global_load_lds_dwordx4 v20, s[54:55] offset:0\n"
    "  global_load_lds_dwordx4 v21, s[54:55] offset:1024\n"
    "  global_load_lds_dwordx4 v22, s[54:55] offset:2048\n"
    "  global_load_lds_dwordx4 v23, s[54:55] offset:3072\n"
    "  s_mov_b32 m0, 65536\n"
    "  s_nop 0\n"
    "  global_load_lds_dwordx4 v24, s[54:55] offset:0\n"
    "  s_waitcnt vmcnt(10)\n"
    "  ds_read_b64_tr_b16 v[156:157], v29 offset:5120\n"
    "  ds_read_b64_tr_b16 v[158:159], v29 offset:7680\n"
    "  ds_read_b64_tr_b16 v[160:161], v29 offset:5152\n"
    "  ds_read_b64_tr_b16 v[162:163], v29 offset:7712\n"
    "  ds_read_b64_tr_b16 v[164:165], v29 offset:5184\n"
    "  ds_read_b64_tr_b16 v[166:167], v29 offset:7744\n"
    "  ds_read_b64_tr_b16 v[168:169], v29 offset:5216\n"
    "  ds_read_b64_tr_b16 v[170:171], v29 offset:7776\n"
    "  ds_read_b64_tr_b16 v[172:173], v29 offset:5248\n"
    "  ds_read_b64_tr_b16 v[174:175], v29 offset:7808\n"
    "  s_waitcnt lgkmcnt(0)\n"
    "  s_add_u32 s54, s16, 163840\n"
    "  s_addc_u32 s55, s17, 0\n"
    "  s_mov_b32 m0, 66560\n"
    "  s_nop 0\n"
    "  global_load_lds_dwordx4 v20, s[54:55] offset:0\n"
    "  global_load_lds_dwordx4 v21, s[54:55] offset:1024\n"
    "  global_load_lds_dwordx4 v22, s[54:55] offset:2048\n"
    "  global_load_lds_dwordx4 v23, s[54:55] offset:3072\n"
    "  s_mov_b32 m0, 70656\n"
    "  s_nop 0\n"
    "  global_load_lds_dwordx4 v24, s[54:55] offset:0\n"
    "  s_waitcnt vmcnt(10)\n"
    "  ds_read_b64_tr_b16 v[176:177], v25 offset:20480\n"
    "  ds_read_b64_tr_b16 v[178:179], v25 offset:23040\n"
    "  ds_read_b64_tr_b16 v[180:181], v25 offset:20512\n"
    "  ds_read_b64_tr_b16 v[182:183], v25 offset:23072\n"
    "  ds_read_b64_tr_b16 v[184:185], v25 offset:20544\n"
    "  ds_read_b64_tr_b16 v[186:187], v25 offset:23104\n"
    "  ds_read_b64_tr_b16 v[188:189], v25 offset:20576\n"
    "  ds_read_b64_tr_b16 v[190:191], v25 offset:23136\n"
    "  ds_read_b64_tr_b16 v[192:193], v25 offset:20608\n"
    "  ds_read_b64_tr_b16 v[194:195], v25 offset:23168\n"
    "  s_waitcnt lgkmcnt(0)\n"
    "  s_add_u32 s54, s16, 184320\n"
    "  s_addc_u32 s55, s17, 0\n"
    "  s_mov_b32 m0, 20480\n"
    "  s_nop 0\n"
    "  global_load_lds_dwordx4 v20, s[54:55] offset:0\n"
    "  global_load_lds_dwordx4 v21, s[54:55] offset:1024\n"
    "  global_load_lds_dwordx4 v22, s[54:55] offset:2048\n"
    "  global_load_lds_dwordx4 v23, s[54:55] offset:3072\n"
    "  s_mov_b32 m0, 24576\n"
    "  s_nop 0\n"
    "  global_load_lds_dwordx4 v24, s[54:55] offset:0\n"
    "  v_add_u32 v253, 61440, v25\n"
    "  v_add_u32 v254, 66560, v25\n"
    "  v_add_u32 v255, 20480, v25\n"
    "  s_mov_b32 s60, 1\n"
    "  s_branch .Ledm_G2\n"
    ".Ledm_Em:\n"
    "  s_setprio 0\n"
    "  s_lshl_b32 s23, s21, 8\n"
    "  s_add_u32 s23, s23, 153600\n"
    "  v_lshl_add_u32 v49, v3, 2, s23\n"
    "  v_and_b32 v48, 3, v3\n"
    "  v_mul_u32_u24 v48, 0x104, v48\n"
    "  v_add_u32 v48, 153600, v48\n"
    "  s_mul_i32 s23, s21, 160\n"
    "  s_add_u32 s23, s23, 3072\n"
    "  v_add_u32 v26, 0, v3\n"
    "  v_mul_u32_u24 v27, 6554, v26\n"
    "  v_lshrrev_b32 v27, 16, v27\n"
    "  v_lshlrev_b32 v26, 4, v26\n"
    "  v_mul_u32_u24 v27, 480, v27\n"
    "  v_add3_u32 v20, v26, v27, s23\n"
    "  v_add_u32 v26, 64, v3\n"
    "  v_mul_u32_u24 v27, 6554, v26\n"
    "  v_lshrrev_b32 v27, 16, v27\n"
    "  v_lshlrev_b32 v26, 4, v26\n"
    "  v_mul_u32_u24 v27, 480, v27\n"
    "  v_add3_u32 v21, v26, v27, s23\n"
    "  v_subrev_u32 v21, 1024, v21\n"
    "  v_add_u32 v26, 128, v3\n"
    "  v_mul_u32_u24 v27, 6554, v26\n"
    "  v_lshrrev_b32 v27, 16, v27\n"
    "  v_lshlrev_b32 v26, 4, v26\n"
    "  v_mul_u32_u24 v27, 480, v27\n"
    "  v_add3_u32 v22, v26, v27, s23\n"
    "  v_subrev_u32 v22, 2048, v22\n"
    "  v_add_u32 v26, 192, v3\n"
    "  v_mul_u32_u24 v27, 6554, v26\n"
    "  v_lshrrev_b32 v27, 16, v27\n"
    "  v_lshlrev_b32 v26, 4, v26\n"
    "  v_mul_u32_u24 v27, 480, v27\n"
    "  v_add3_u32 v23, v26, v27, s23\n"
    "  v_subrev_u32 v23, 3072, v23\n"
    "  v_add_u32 v26, 256, v3\n"
    "  v_mul_u32_u24 v27, 6554, v26\n"
    "  v_lshrrev_b32 v27, 16, v27\n"
    "  v_lshlrev_b32 v26, 4, v26\n"
    "  v_mul_u32_u24 v27, 480, v27\n"
    "  v_add3_u32 v24, v26, v27, s23\n"
    "  v_lshrrev_b32 v26, 2, v3\n"
    "  v_mul_u32_u24 v26, 160, v26\n"
    "  v_and_b32 v27, 3, v3\n"
    "  v_lshl_add_u32 v25, v27, 3, v26\n"
    "  v_add_u32 v29, 61440, v25\n"
    "  v_add_u32 v9, 137216, v25\n"
    "  s_sub_u32 s16, s16, 3072\n"
    "  s_subb_u32 s17, s17, 0\n"
    "  v_lshrrev_b32 v27, 2, v3\n"
    "  v_lshlrev_b32 v26, 7, v27\n"
    "  v_bfe_u32 v28, v3, 3, 2\n"
    "  v_lshl_or_b32 v26, v28, 5, v26\n"
    "  v_and_b32 v28, 3, v3\n"
    "  v_lshl_or_b32 v26, v28, 3, v26\n"
    "  s_mul_i32 s26, s21, 20480\n"
    "  s_add_u32 s26, s26, 71680\n"
    "  v_add_u32 v53, s26, v26\n"
    "  v_bfe_u32 v27, v3, 2, 2\n"
    "  v_mul_u32_u24 v46, 0x120, v27\n"
    "  v_lshrrev_b32 v28, 5, v3\n"
    "  v_lshl_or_b32 v46, v28, 7, v46\n"
    "  v_bfe_u32 v28, v3, 4, 1\n"
    "  v_lshl_or_b32 v46, v28, 10, v46\n"
    "  v_and_b32 v28, 3, v3\n"
    "  v_lshl_or_b32 v46, v28, 3, v46\n"
    "  v_add_u32 v46, s26, v46\n"
    "  v_lshrrev_b32 v27, 2, v3\n"
    "  v_lshlrev_b32 v28, 4, v28\n"
    "  s_mul_i32 s23, s21, 80\n"
    "  v_add_u32 v54, s23, v27\n"
    "  v_lshl_or_b32 v54, v54, 15, v28\n"
    "  v_and_b32 v27, 3, v27\n"
    "  v_lshrrev_b32 v28, 4, v3\n"
    "  v_mul_u32_u24 v47, 0x120, v28\n"
    "  v_lshl_or_b32 v47, v27, 3, v47\n"
    "  v_and_b32 v28, 1, v3\n"
    "  v_lshl_or_b32 v47, v28, 7, v47\n"
    "  v_bfe_u32 v28, v3, 1, 1\n"
    "  v_lshlrev_b32 v28, 5, v28\n"
    "  v_xor_b32 v47, v28, v47\n"
    "  v_add_u32 v47, s26, v47\n"
    "  v_lshlrev_b32 v52, 4, v3\n"
    "  s_add_u32 s26, s24, 0\n"
    "  s_add_u32 s40, s18, s26\n"
    "  s_addc_u32 s41, s19, 0\n"
    "  s_add_u32 s26, s24, 524288\n"
    "  s_add_u32 s42, s18, s26\n"
    "  s_addc_u32 s43, s19, 0\n"
    "  s_add_u32 s26, s24, 1048576\n"
    "  s_add_u32 s44, s18, s26\n"
    "  s_addc_u32 s45, s19, 0\n"
    "  s_add_u32 s26, s24, 1572864\n"
    "  s_add_u32 s46, s18, s26\n"
    "  s_addc_u32 s47, s19, 0\n"
    "  s_add_u32 s26, s24, 2097152\n"
    "  s_add_u32 s48, s18, s26\n"
    "  s_addc_u32 s49, s19, 0\n"
    "  s_waitcnt lgkmcnt(0)\n"
    "  s_lshl_b32 s23, s28, 4\n"
    "  s_lshr_b32 s38, s38, s23\n"
    "  s_lshl_b32 s38, s38, 16\n"
    "  v_mul_f32 v26, s38, s38\n"
    "  v_cvt_pk_bf16_f32 v26, v26, v26\n"
    "  v_lshlrev_b32 v26, 16, v26\n"
    "  v_add_f32 v26, 0x3e800000, v26\n"
    "  v_cvt_pk_bf16_f32 v26, v26, v26\n"
    "  v_lshlrev_b32 v26, 16, v26\n"
    "  v_rcp_f32 v26, v26\n"
    "  s_nop 0\n"
    "  v_mul_f32 v26, 0x3e800000, v26\n"
    "  v_cvt_pk_bf16_f32 v26, v26, v26\n"
    "  v_and_b32 v26, 0xffff, v26\n"
    "  v_and_b32 v27, 1, v3\n"
    "  v_lshlrev_b32 v27, 4, v27\n"
    "  v_lshlrev_b32 v26, v27, v26\n"
    "  v_bfe_u32 v27, v3, 1, 3\n"
    "  v_lshrrev_b32 v28, 4, v3\n"
    "  v_lshlrev_b32 v28, 1, v28\n"
    "  v_cmp_eq_u32_e32 vcc, v27, v28\n"
    "  s_nop 1\n"
    "  v_cndmask_b32_e32 v50, 0, v26, vcc\n"
    "  v_add_u32 v28, 1, v28\n"
    "  v_cmp_eq_u32_e32 vcc, v27, v28\n"
    "  s_nop 1\n"
    "  v_cndmask_b32_e32 v51, 0, v26, vcc\n"
    "  s_waitcnt vmcnt(0)\n"
    ".Ledm_es9:\n"
    "  s_sleep 4\n"
    "  s_sub_u32 s39, s39, 1\n"
    "  s_cbranch_scc1 .Ledm_abort\n"
    "  ds_read_b32 v55, v48 offset:0\n"
    "  s_waitcnt lgkmcnt(0)\n"
    "  v_cmp_gt_u32_e32 vcc, 6, v55\n"
    "  s_cbranch_vccnz .Ledm_es9\n"
    "  s_cmp_eq_u32 s21, 0\n"
    "  s_cbranch_scc1 .Ledm_E0\n"
    "  s_cmp_eq_u32 s21, 1\n"
    "  s_cbranch_scc1 .Ledm_E1\n"
    ".Ledm_pl10:\n"
    "  s_sub_u32 s39, s39, 1\n"
    "  s_cbranch_scc1 .Ledm_abort\n"
    "  ds_read_b32 v55, v48 offset:0\n"
    "  s_waitcnt lgkmcnt(0)\n"
    "  v_cmp_gt_u32_e32 vcc, 16, v55\n"
    "  s_cbranch_vccnz .Ledm_pl10\n"
    "  s_add_u32 s54, s16, 0\n"
    "  s_addc_u32 s55, s17, 0\n"
    "  s_mov_b32 m0, 5120\n"
    "  s_nop 0\n"
    "  global_load_lds_dwordx4 v20, s[54:55] offset:0\n"
    "  global_load_lds_dwordx4 v21, s[54:55] offset:1024\n"
    "  global_load_lds_dwordx4 v22, s[54:55] offset:2048\n"
    "  global_load_lds_dwordx4 v23, s[54:55] offset:3072\n"
    "  s_mov_b32 m0, 9216\n"
    "  s_nop 0\n"
    "  global_load_lds_dwordx4 v24, s[54:55] offset:0\n"
    "  s_add_u32 s54, s16, 20480\n"
    "  s_addc_u32 s55, s17, 0\n"
    "  s_mov_b32 m0, 15360\n"
    "  s_nop 0\n"
    "  global_load_lds_dwordx4 v20, s[54:55] offset:0\n"
    "  global_load_lds_dwordx4 v21, s[54:55] offset:1024\n"
    "  global_load_lds_dwordx4 v22, s[54:55] offset:2048\n"
    "  global_load_lds_dwordx4 v23, s[54:55] offset:3072\n"
    "  s_mov_b32 m0, 19456\n"
    "  s_nop 0\n"
    "  global_load_lds_dwordx4 v24, s[54:55] offset:0\n"
    "  s_waitcnt vmcnt(5)\n"
    "  ds_read_b64_tr_b16 v[56:57], v25 offset:5120\n"
    "  ds_read_b64_tr_b16 v[58:59], v25 offset:7680\n"
    "  ds_read_b64_tr_b16 v[60:61], v25 offset:5152\n"
    "  ds_read_b64_tr_b16 v[62:63], v25 offset:7712\n"
    "  ds_read_b64_tr_b16 v[64:65], v25 offset:5184\n"
    "  ds_read_b64_tr_b16 v[66:67], v25 offset:7744\n"
    "  ds_read_b64_tr_b16 v[68:69], v25 offset:5216\n"
    "  ds_read_b64_tr_b16 v[70:71], v25 offset:7776\n"
    "  ds_read_b64_tr_b16 v[72:73], v25 offset:5248\n"
    "  ds_read_b64_tr_b16 v[74:75], v25 offset:7808\n"
    "  s_waitcnt lgkmcnt(0)\n"
    "  s_add_u32 s54, s16, 40960\n"
    "  s_addc_u32 s55, s17, 0\n"
    "  s_mov_b32 m0, 5120\n"
    "  s_nop 0\n"
    "  global_load_lds_dwordx4 v20, s[54:55] offset:0\n"
    "  global_load_lds_dwordx4 v21, s[54:55] offset:1024\n"
    "  global_load_lds_dwordx4 v22, s[54:55] offset:2048\n"
    "  global_load_lds_dwordx4 v23, s[54:55] offset:3072\n"
    "  s_mov_b32 m0, 9216\n"
    "  s_nop 0\n"
    "  global_load_lds_dwordx4 v24, s[54:55] offset:0\n"
    "  s_waitcnt vmcnt(5)\n"
    "  ds_read_b64_tr_b16 v[76:77], v25 offset:15360\n"
    "  ds_read_b64_tr_b16 v[78:79], v25 offset:17920\n"
    "  ds_read_b64_tr_b16 v[80:81], v25 offset:15392\n"
    "  ds_read_b64_tr_b16 v[82:83], v25 offset:17952\n"
    "  ds_read_b64_tr_b16 v[84:85], v25 offset:15424\n"
    "  ds_read_b64_tr_b16 v[86:87], v25 offset:17984\n"
    "  ds_read_b64_tr_b16 v[88:89], v25 offset:15456\n"
    "  ds_read_b64_tr_b16 v[90:91], v25 offset:18016\n"
    "  ds_read_b64_tr_b16 v[92:93], v25 offset:15488\n"
    "  ds_read_b64_tr_b16 v[94:95], v25 offset:18048\n"
    "  s_waitcnt lgkmcnt(0)\n"
    "  s_add_u32 s54, s16, 61440\n"
    "  s_addc_u32 s55, s17, 0\n"
    "  s_mov_b32 m0, 15360\n"
    "  s_nop 0\n"
    "  global_load_lds_dwordx4 v20, s[54:55] offset:0\n"
    "  global_load_lds_dwordx4 v21, s[54:55] offset:1024\n"
    "  global_load_lds_dwordx4 v22, s[54:55] offset:2048\n"
    "  global_load_lds_dwordx4 v23, s[54:55] offset:3072\n"
    "  s_mov_b32 m0, 19456\n"
    "  s_nop 0\n"
    "  global_load_lds_dwordx4 v24, s[54:55] offset:0\n"
    ".Ledm_pl11:\n"
    "  s_sub_u32 s39, s39, 1\n"
    "  s_cbranch_scc1 .Ledm_abort\n"
    "  ds_read_b32 v55, v48 offset:0\n"
    "  s_waitcnt lgkmcnt(0)\n"
    "  v_cmp_gt_u32_e32 vcc, 18, v55\n"
    "  s_cbranch_vccnz .Ledm_pl11\n"
    "  s_add_u32 s54, s16, 81920\n"
    "  s_addc_u32 s55, s17, 0\n"
    "  s_mov_b32 m0, 30720\n"
    "  s_nop 0\n"
    "  global_load_lds_dwordx4 v20, s[54:55] offset:0\n"
    "  global_load_lds_dwordx4 v21, s[54:55] offset:1024\n"
    "  global_load_lds_dwordx4 v22, s[54:55] offset:2048\n"
    "  global_load_lds_dwordx4 v23, s[54:55] offset:3072\n"
    "  s_mov_b32 m0, 34816\n"
    "  s_nop 0\n"
    "  global_load_lds_dwordx4 v24, s[54:55] offset:0\n"
    "  s_waitcnt vmcnt(10)\n"
    "  ds_read_b64_tr_b16 v[96:97], v25 offset:5120\n"
    "  ds_read_b64_tr_b16 v[98:99], v25 offset:7680\n"
    "  ds_read_b64_tr_b16 v[100:101], v25 offset:5152\n"
    "  ds_read_b64_tr_b16 v[102:103], v25 offset:7712\n"
    "  ds_read_b64_tr_b16 v[104:105], v25 offset:5184\n"
    "  ds_read_b64_tr_b16 v[106:107], v25 offset:7744\n"
    "  ds_read_b64_tr_b16 v[108:109], v25 offset:5216\n"
    "  ds_read_b64_tr_b16 v[110:111], v25 offset:7776\n"
    "  ds_read_b64_tr_b16 v[112:113], v25 offset:5248\n"
    "  ds_read_b64_tr_b16 v[114:115], v25 offset:7808\n"
    "  s_waitcnt lgkmcnt(0)\n"
    "  s_add_u32 s54, s16, 102400\n"
    "  s_addc_u32 s55, s17, 0\n"
    "  s_mov_b32 m0, 5120\n"
    "  s_nop 0\n"
    "  global_load_lds_dwordx4 v20, s[54:55] offset:0\n"
    "  global_load_lds_dwordx4 v21, s[54:55] offset:1024\n"
    "  global_load_lds_dwordx4 v22, s[54:55] offset:2048\n"
    "  global_load_lds_dwordx4 v23, s[54:55] offset:3072\n"
    "  s_mov_b32 m0, 9216\n"
    "  s_nop 0\n"
    "  global_load_lds_dwordx4 v24, s[54:55] offset:0\n"
    "  s_waitcnt vmcnt(10)\n"
    "  ds_read_b64_tr_b16 v[116:117], v25 offset:15360\n"
    "  ds_read_b64_tr_b16 v[118:119], v25 offset:17920\n"
    "  ds_read_b64_tr_b16 v[120:121], v25 offset:15392\n"
    "  ds_read_b64_tr_b16 v[122:123], v25 offset:17952\n"
    "  ds_read_b64_tr_b16 v[124:125], v25 offset:15424\n"
    "  ds_read_b64_tr_b16 v[126:127], v25 offset:17984\n"
    "  ds_read_b64_tr_b16 v[128:129], v25 offset:15456\n"
    "  ds_read_b64_tr_b16 v[130:131], v25 offset:18016\n"
    "  ds_read_b64_tr_b16 v[132:133], v25 offset:15488\n"
    "  ds_read_b64_tr_b16 v[134:135], v25 offset:18048\n"
    "  s_waitcnt lgkmcnt(0)\n"
    "  s_add_u32 s54, s16, 122880\n"
    "  s_addc_u32 s55, s17, 0\n"
    "  s_mov_b32 m0, 15360\n"
    "  s_nop 0\n"
    "  global_load_lds_dwordx4 v20, s[54:55] offset:0\n"
    "  global_load_lds_dwordx4 v21, s[54:55] offset:1024\n"
    "  global_load_lds_dwordx4 v22, s[54:55] offset:2048\n"
    "  global_load_lds_dwordx4 v23, s[54:55] offset:3072\n"
    "  s_mov_b32 m0, 19456\n"
    "  s_nop 0\n"
    "  global_load_lds_dwordx4 v24, s[54:55] offset:0\n"
    "  s_waitcnt vmcnt(10)\n"
    "  ds_read_b64_tr_b16 v[136:137], v25 offset:30720\n"
    "  ds_read_b64_tr_b16 v[138:139], v25 offset:33280\n"
    "  ds_read_b64_tr_b16 v[140:141], v25 offset:30752\n"
    "  ds_read_b64_tr_b16 v[142:143], v25 offset:33312\n"
    "  ds_read_b64_tr_b16 v[144:145], v25 offset:30784\n"
    "  ds_read_b64_tr_b16 v[146:147], v25 offset:33344\n"
    "  ds_read_b64_tr_b16 v[148:149], v25 offset:30816\n"
    "  ds_read_b64_tr_b16 v[150:151], v25 offset:33376\n"
    "  ds_read_b64_tr_b16 v[152:153], v25 offset:30848\n"
    "  ds_read_b64_tr_b16 v[154:155], v25 offset:33408\n"
    "  s_waitcnt lgkmcnt(0)\n"
    "  s_add_u32 s54, s16, 143360\n"
    "  s_addc_u32 s55, s17, 0\n"
    "  s_mov_b32 m0, 30720\n"
    "  s_nop 0\n"
    "  global_load_lds_dwordx4 v20, s[54:55] offset:0\n"
    "  global_load_lds_dwordx4 v21, s[54:55] offset:1024\n"
    "  global_load_lds_dwordx4 v22, s[54:55] offset:2048\n"
    "  global_load_lds_dwordx4 v23, s[54:55] offset:3072\n"
    "  s_mov_b32 m0, 34816\n"
    "  s_nop 0\n"
    "  global_load_lds_dwordx4 v24, s[54:55] offset:0\n"
    "  s_waitcnt vmcnt(10)\n"
    "  ds_read_b64_tr_b16 v[156:157], v25 offset:5120\n"
    "  ds_read_b64_tr_b16 v[158:159], v25 offset:7680\n"
    "  ds_read_b64_tr_b16 v[160:161], v25 offset:5152\n"
    "  ds_read_b64_tr_b16 v[162:163], v25 offset:7712\n"
    "  ds_read_b64_tr_b16 v[164:165], v25 offset:5184\n"
    "  ds_read_b64_tr_b16 v[166:167], v25 offset:7744\n"
    "  ds_read_b64_tr_b16 v[168:169], v25 offset:5216\n"
    "  ds_read_b64_tr_b16 v[170:171], v25 offset:7776\n"
    "  ds_read_b64_tr_b16 v[172:173], v25 offset:5248\n"
    "  ds_read_b64_tr_b16 v[174:175], v25 offset:7808\n"
    "  s_waitcnt lgkmcnt(0)\n"
    "  s_add_u32 s54, s16, 163840\n"
    "  s_addc_u32 s55, s17, 0\n"
    "  s_mov_b32 m0, 5120\n"
    "  s_nop 0\n"
    "  global_load_lds_dwordx4 v20, s[54:55] offset:0\n"
    "  global_load_lds_dwordx4 v21, s[54:55] offset:1024\n"
    "  global_load_lds_dwordx4 v22, s[54:55] offset:2048\n"
    "  global_load_lds_dwordx4 v23, s[54:55] offset:3072\n"
    "  s_mov_b32 m0, 9216\n"
    "  s_nop 0\n"
    "  global_load_lds_dwordx4 v24, s[54:55] offset:0\n"
    "  s_waitcnt vmcnt(10)\n"
    "  ds_read_b64_tr_b16 v[176:177], v25 offset:15360\n"
    "  ds_read_b64_tr_b16 v[178:179], v25 offset:17920\n"
    "  ds_read_b64_tr_b16 v[180:181], v25 offset:15392\n"
    "  ds_read_b64_tr_b16 v[182:183], v25 offset:17952\n"
    "  ds_read_b64_tr_b16 v[184:185], v25 offset:15424\n"
    "  ds_read_b64_tr_b16 v[186:187], v25 offset:17984\n"
    "  ds_read_b64_tr_b16 v[188:189], v25 offset:15456\n"
    "  ds_read_b64_tr_b16 v[190:191], v25 offset:18016\n"
    "  ds_read_b64_tr_b16 v[192:193], v25 offset:15488\n"
    "  ds_read_b64_tr_b16 v[194:195], v25 offset:18048\n"
    "  s_waitcnt lgkmcnt(0)\n"
    "  s_add_u32 s54, s16, 184320\n"
    "  s_addc_u32 s55, s17, 0\n"
    "  s_mov_b32 m0, 15360\n"
    "  s_nop 0\n"
    "  global_load_lds_dwordx4 v20, s[54:55] offset:0\n"
    "  global_load_lds_dwordx4 v21, s[54:55] offset:1024\n"
    "  global_load_lds_dwordx4 v22, s[54:55] offset:2048\n"
    "  global_load_lds_dwordx4 v23, s[54:55] offset:3072\n"
    "  s_mov_b32 m0, 19456\n"
    "  s_nop 0\n"
    "  global_load_lds_dwordx4 v24, s[54:55] offset:0\n"
    "  v_add_u32 v253, 30720, v25\n"
    "  v_add_u32 v254, 5120, v25\n"
    "  v_add_u32 v255, 15360, v25\n"
    "  s_mov_b32 s60, 1\n"
    "  s_branch .Ledm_G2\n"
    ".Ledm_E1:\n"
    ".Ledm_pl12:\n"
    "  s_sub_u32 s39, s39, 1\n"
    "  s_cbranch_scc1 .Ledm_abort\n"
    "  ds_read_b32 v55, v48 offset:0\n"
    "  s_waitcnt lgkmcnt(0)\n"
    "  v_cmp_gt_u32_e32 vcc, 16, v55\n"
    "  s_cbranch_vccnz .Ledm_pl12\n"
    "  s_add_u32 s54, s16, 0\n"
    "  s_addc_u32 s55, s17, 0\n"
    "  s_mov_b32 m0, 0\n"
    "  s_nop 0\n"
    "  global_load_lds_dwordx4 v20, s[54:55] offset:0\n"
    "  global_load_lds_dwordx4 v21, s[54:55] offset:1024\n"
    "  global_load_lds_dwordx4 v22, s[54:55] offset:2048\n"
    "  global_load_lds_dwordx4 v23, s[54:55] offset:3072\n"
    "  s_mov_b32 m0, 4096\n"
    "  s_nop 0\n"
    "  global_load_lds_dwordx4 v24, s[54:55] offset:0\n"
    "  s_add_u32 s54, s16, 20480\n"
    "  s_addc_u32 s55, s17, 0\n"
    "  s_mov_b32 m0, 10240\n"
    "  s_nop 0\n"
    "  global_load_lds_dwordx4 v20, s[54:55] offset:0\n"
    "  global_load_lds_dwordx4 v21, s[54:55] offset:1024\n"
    "  global_load_lds_dwordx4 v22, s[54:55] offset:2048\n"
    "  global_load_lds_dwordx4 v23, s[54:55] offset:3072\n"
    "  s_mov_b32 m0, 14336\n"
    "  s_nop 0\n"
    "  global_load_lds_dwordx4 v24, s[54:55] offset:0\n"
    "  s_waitcnt vmcnt(5)\n"
    "  ds_read_b64_tr_b16 v[56:57], v25 offset:0\n"
    "  ds_read_b64_tr_b16 v[58:59], v25 offset:2560\n"
    "  ds_read_b64_tr_b16 v[60:61], v25 offset:32\n"
    "  ds_read_b64_tr_b16 v[62:63], v25 offset:2592\n"
    "  ds_read_b64_tr_b16 v[64:65], v25 offset:64\n"
    "  ds_read_b64_tr_b16 v[66:67], v25 offset:2624\n"
    "  ds_read_b64_tr_b16 v[68:69], v25 offset:96\n"
    "  ds_read_b64_tr_b16 v[70:71], v25 offset:2656\n"
    "  ds_read_b64_tr_b16 v[72:73], v25 offset:128\n"
    "  ds_read_b64_tr_b16 v[74:75], v25 offset:2688\n"
    "  s_waitcnt lgkmcnt(0)\n"
    "  s_add_u32 s54, s16, 40960\n"
    "  s_addc_u32 s55, s17, 0\n"
    "  s_mov_b32 m0, 0\n"
    "  s_nop 0\n"
    "  global_load_lds_dwordx4 v20, s[54:55] offset:0\n"
    "  global_load_lds_dwordx4 v21, s[54:55] offset:1024\n"
    "  global_load_lds_dwordx4 v22, s[54:55] offset:2048\n"
    "  global_load_lds_dwordx4 v23, s[54:55] offset:3072\n"
    "  s_mov_b32 m0, 4096\n"
    "  s_nop 0\n"
    "  global_load_lds_dwordx4 v24, s[54:55] offset:0\n"
    "  s_waitcnt vmcnt(5)\n"
    "  ds_read_b64_tr_b16 v[76:77], v25 offset:10240\n"
    "  ds_read_b64_tr_b16 v[78:79], v25 offset:12800\n"
    "  ds_read_b64_tr_b16 v[80:81], v25 offset:10272\n"
    "  ds_read_b64_tr_b16 v[82:83], v25 offset:12832\n"
    "  ds_read_b64_tr_b16 v[84:85], v25 offset:10304\n"
    "  ds_read_b64_tr_b16 v[86:87], v25 offset:12864\n"
    "  ds_read_b64_tr_b16 v[88:89], v25 offset:10336\n"
    "  ds_read_b64_tr_b16 v[90:91], v25 offset:12896\n"
    "  ds_read_b64_tr_b16 v[92:93], v25 offset:10368\n"
    "  ds_read_b64_tr_b16 v[94:95], v25 offset:12928\n"
    "  s_waitcnt lgkmcnt(0)\n"
    "  s_add_u32 s54, s16, 61440\n"
    "  s_addc_u32 s55, s17, 0\n"
    "  s_mov_b32 m0, 10240\n"
    "  s_nop 0\n"
    "  global_load_lds_dwordx4 v20, s[54:55] offset:0\n"
    "  global_load_lds_dwordx4 v21, s[54:55] offset:1024\n"
    "  global_load_lds_dwordx4 v22, s[54:55] offset:2048\n"
    "  global_load_lds_dwordx4 v23, s[54:55] offset:3072\n"
    "  s_mov_b32 m0, 14336\n"
    "  s_nop 0\n"
    "  global_load_lds_dwordx4 v24, s[54:55] offset:0\n"
    ".Ledm_pl13:\n"
    "  s_sub_u32 s39, s39, 1\n"
    "  s_cbranch_scc1 .Ledm_abort\n"
    "  ds_read_b32 v55, v48 offset:0\n"
    "  s_waitcnt lgkmcnt(0)\n"
    "  v_cmp_gt_u32_e32 vcc, 18, v55\n"
    "  s_cbranch_vccnz .Ledm_pl13\n"
    "  s_add_u32 s54, s16, 81920\n"
    "  s_addc_u32 s55, s17, 0\n"
    "  s_mov_b32 m0, 25600\n"
    "  s_nop 0\n"
    "  global_load_lds_dwordx4 v20, s[54:55] offset:0\n"
    "  global_load_lds_dwordx4 v21, s[54:55] offset:1024\n"
    "  global_load_lds_dwordx4 v22, s[54:55] offset:2048\n"
    "  global_load_lds_dwordx4 v23, s[54:55] offset:3072\n"
    "  s_mov_b32 m0, 29696\n"
    "  s_nop 0\n"
    "  global_load_lds_dwordx4 v24, s[54:55] offset:0\n"
    "  s_waitcnt vmcnt(10)\n"
    "  ds_read_b64_tr_b16 v[96:97], v25 offset:0\n"
    "  ds_read_b64_tr_b16 v[98:99], v25 offset:2560\n"
    "  ds_read_b64_tr_b16 v[100:101], v25 offset:32\n"
    "  ds_read_b64_tr_b16 v[102:103], v25 offset:2592\n"
    "  ds_read_b64_tr_b16 v[104:105], v25 offset:64\n"
    "  ds_read_b64_tr_b16 v[106:107], v25 offset:2624\n"
    "  ds_read_b64_tr_b16 v[108:109], v25 offset:96\n"
    "  ds_read_b64_tr_b16 v[110:111], v25 offset:2656\n"
    "  ds_read_b64_tr_b16 v[112:113], v25 offset:128\n"
    "  ds_read_b64_tr_b16 v[114:115], v25 offset:2688\n"
    "  s_waitcnt lgkmcnt(0)\n"
    "  s_add_u32 s54, s16, 102400\n"
    "  s_addc_u32 s55, s17, 0\n"
    "  s_mov_b32 m0, 0\n"
    "  s_nop 0\n"
    "  global_load_lds_dwordx4 v20, s[54:55] offset:0\n"
    "  global_load_lds_dwordx4 v21, s[54:55] offset:1024\n"
    "  global_load_lds_dwordx4 v22, s[54:55] offset:2048\n"
    "  global_load_lds_dwordx4 v23, s[54:55] offset:3072\n"
    "  s_mov_b32 m0, 4096\n"
    "  s_nop 0\n"
    "  global_load_lds_dwordx4 v24, s[54:55] offset:0\n"
    "  s_waitcnt vmcnt(10)\n"
    "  ds_read_b64_tr_b16 v[116:117], v25 offset:10240\n"
    "  ds_read_b64_tr_b16 v[118:119], v25 offset:12800\n"
    "  ds_read_b64_tr_b16 v[120:121], v25 offset:10272\n"
    "  ds_read_b64_tr_b16 v[122:123], v25 offset:12832\n"
    "  ds_read_b64_tr_b16 v[124:125], v25 offset:10304\n"
    "  ds_read_b64_tr_b16 v[126:127], v25 offset:12864\n"
    "  ds_read_b64_tr_b16 v[128:129], v25 offset:10336\n"
    "  ds_read_b64_tr_b16 v[130:131], v25 offset:12896\n"
    "  ds_read_b64_tr_b16 v[132:133], v25 offset:10368\n"
    "  ds_read_b64_tr_b16 v[134:135], v25 offset:12928\n"
    "  s_waitcnt lgkmcnt(0)\n"
    "  s_add_u32 s54, s16, 122880\n"
    "  s_addc_u32 s55, s17, 0\n"
    "  s_mov_b32 m0, 10240\n"
    "  s_nop 0\n"
    "  global_load_lds_dwordx4 v20, s[54:55] offset:0\n"
    "  global_load_lds_dwordx4 v21, s[54:55] offset:1024\n"
    "  global_load_lds_dwordx4 v22, s[54:55] offset:2048\n"
    "  global_load_lds_dwordx4 v23, s[54:55] offset:3072\n"
    "  s_mov_b32 m0, 14336\n"
    "  s_nop 0\n"
    "  global_load_lds_dwordx4 v24, s[54:55] offset:0\n"
    "  s_waitcnt vmcnt(10)\n"
    "  ds_read_b64_tr_b16 v[136:137], v25 offset:25600\n"
    "  ds_read_b64_tr_b16 v[138:139], v25 offset:28160\n"
    "  ds_read_b64_tr_b16 v[140:141], v25 offset:25632\n"
    "  ds_read_b64_tr_b16 v[142:143], v25 offset:28192\n"
    "  ds_read_b64_tr_b16 v[144:145], v25 offset:25664\n"
    "  ds_read_b64_tr_b16 v[146:147], v25 offset:28224\n"
    "  ds_read_b64_tr_b16 v[148:149], v25 offset:25696\n"
    "  ds_read_b64_tr_b16 v[150:151], v25 offset:28256\n"
    "  ds_read_b64_tr_b16 v[152:153], v25 offset:25728\n"
    "  ds_read_b64_tr_b16 v[154:155], v25 offset:28288\n"
    "  s_waitcnt lgkmcnt(0)\n"
    "  s_add_u32 s54, s16, 143360\n"
    "  s_addc_u32 s55, s17, 0\n"
    "  s_mov_b32 m0, 25600\n"
    "  s_nop 0\n"
    "  global_load_lds_dwordx4 v20, s[54:55] offset:0\n"
    "  global_load_lds_dwordx4 v21, s[54:55] offset:1024\n"
    "  global_load_lds_dwordx4 v22, s[54:55] offset:2048\n"
    "  global_load_lds_dwordx4 v23, s[54:55] offset:3072\n"
    "  s_mov_b32 m0, 29696\n"
    "  s_nop 0\n"
    "  global_load_lds_dwordx4 v24, s[54:55] offset:0\n"
    "  s_waitcnt vmcnt(10)\n"
    "  ds_read_b64_tr_b16 v[156:157], v25 offset:0\n"
    "  ds_read_b64_tr_b16 v[158:159], v25 offset:2560\n"
    "  ds_read_b64_tr_b16 v[160:161], v25 offset:32\n"
    "  ds_read_b64_tr_b16 v[162:163], v25 offset:2592\n"
    "  ds_read_b64_tr_b16 v[164:165], v25 offset:64\n"
    "  ds_read_b64_tr_b16 v[166:167], v25 offset:2624\n"
    "  ds_read_b64_tr_b16 v[168:169], v25 offset:96\n"
    "  ds_read_b64_tr_b16 v[170:171], v25 offset:2656\n"
    "  ds_read_b64_tr_b16 v[172:173], v25 offset:128\n"
    "  ds_read_b64_tr_b16 v[174:175], v25 offset:2688\n"
    "  s_waitcnt lgkmcnt(0)\n"
    "  s_add_u32 s54, s16, 163840\n"
    "  s_addc_u32 s55, s17, 0\n"
    "  s_mov_b32 m0, 0\n"
    "  s_nop 0\n"
    "  global_load_lds_dwordx4 v20, s[54:55] offset:0\n"
    "  global_load_lds_dwordx4 v21, s[54:55] offset:1024\n"
    "  global_load_lds_dwordx4 v22, s[54:55] offset:2048\n"
    "  global_load_lds_dwordx4 v23, s[54:55] offset:3072\n"
    "  s_mov_b32 m0, 4096\n"
    "  s_nop 0\n"
    "  global_load_lds_dwordx4 v24, s[54:55] offset:0\n"
    "  s_waitcnt vmcnt(10)\n"
    "  ds_read_b64_tr_b16 v[176:177], v25 offset:10240\n"
    "  ds_read_b64_tr_b16 v[178:179], v25 offset:12800\n"
    "  ds_read_b64_tr_b16 v[180:181], v25 offset:10272\n"
    "  ds_read_b64_tr_b16 v[182:183], v25 offset:12832\n"
    "  ds_read_b64_tr_b16 v[184:185], v25 offset:10304\n"
    "  ds_read_b64_tr_b16 v[186:187], v25 offset:12864\n"
    "  ds_read_b64_tr_b16 v[188:189], v25 offset:10336\n"
    "  ds_read_b64_tr_b16 v[190:191], v25 offset:12896\n"
    "  ds_read_b64_tr_b16 v[192:193], v25 offset:10368\n"
    "  ds_read_b64_tr_b16 v[194:195], v25 offset:12928\n"
    "  s_waitcnt lgkmcnt(0)\n"
    "  s_add_u32 s54, s16, 184320\n"
    "  s_addc_u32 s55, s17, 0\n"
    "  s_mov_b32 m0, 10240\n"
    "  s_nop 0\n"
    "  global_load_lds_dwordx4 v20, s[54:55] offset:0\n"
    "  global_load_lds_dwordx4 v21, s[54:55] offset:1024\n"
    "  global_load_lds_dwordx4 v22, s[54:55] offset:2048\n"
    "  global_load_lds_dwordx4 v23, s[54:55] offset:3072\n"
    "  s_mov_b32 m0, 14336\n"
    "  s_nop 0\n"
    "  global_load_lds_dwordx4 v24, s[54:55] offset:0\n"
    "  v_add_u32 v253, 25600, v25\n"
    "  v_add_u32 v254, 0, v25\n"
    "  v_add_u32 v255, 10240, v25\n"
    "  s_mov_b32 s60, 1\n"
    "  s_branch .Ledm_G2\n"
    ".Ledm_E0:\n"
    ".Ledm_pl14:\n"
    "  s_sub_u32 s39, s39, 1\n"
    "  s_cbranch_scc1 .Ledm_abort\n"
    "  ds_read_b32 v55, v48 offset:0\n"
    "  s_waitcnt lgkmcnt(0)\n"
    "  v_cmp_gt_u32_e32 vcc, 6, v55\n"
    "  s_cbranch_vccnz .Ledm_pl14\n"
    "  s_add_u32 s54, s16, 0\n"
    "  s_addc_u32 s55, s17, 0\n"
    "  s_mov_b32 m0, 157696\n"
    "  s_nop 0\n"
    "  global_load_lds_dwordx4 v20, s[54:55] offset:0\n"
    "  global_load_lds_dwordx4 v21, s[54:55] offset:1024\n"
    "  global_load_lds_dwordx4 v22, s[54:55] offset:2048\n"
    "  global_load_lds_dwordx4 v23, s[54:55] offset:3072\n"
    "  s_mov_b32 m0, 161792\n"
    "  s_nop 0\n"
    "  global_load_lds_dwordx4 v24, s[54:55] offset:0\n"
    "  s_waitcnt vmcnt(0)\n"
    "  ds_read_b64_tr_b16 v[56:57], v9 offset:20480\n"
    "  ds_read_b64_tr_b16 v[58:59], v9 offset:23040\n"
    "  ds_read_b64_tr_b16 v[60:61], v9 offset:20512\n"
    "  ds_read_b64_tr_b16 v[62:63], v9 offset:23072\n"
    "  ds_read_b64_tr_b16 v[64:65], v9 offset:20544\n"
    "  ds_read_b64_tr_b16 v[66:67], v9 offset:23104\n"
    "  ds_read_b64_tr_b16 v[68:69], v9 offset:20576\n"
    "  ds_read_b64_tr_b16 v[70:71], v9 offset:23136\n"
    "  ds_read_b64_tr_b16 v[72:73], v9 offset:20608\n"
    "  ds_read_b64_tr_b16 v[74:75], v9 offset:23168\n"
    "  s_waitcnt lgkmcnt(0)\n"
    "  s_add_u32 s54, s16, 20480\n"
    "  s_addc_u32 s55, s17, 0\n"
    "  s_mov_b32 m0, 157696\n"
    "  s_nop 0\n"
    "  global_load_lds_dwordx4 v20, s[54:55] offset:0\n"
    "  global_load_lds_dwordx4 v21, s[54:55] offset:1024\n"
    "  global_load_lds_dwordx4 v22, s[54:55] offset:2048\n"
    "  global_load_lds_dwordx4 v23, s[54:55] offset:3072\n"
    "  s_mov_b32 m0, 161792\n"
    "  s_nop 0\n"
    "  global_load_lds_dwordx4 v24, s[54:55] offset:0\n"
    "  s_waitcnt vmcnt(0)\n"
    "  ds_read_b64_tr_b16 v[76:77], v9 offset:20480\n"
    "  ds_read_b64_tr_b16 v[78:79], v9 offset:23040\n"
    "  ds_read_b64_tr_b16 v[80:81], v9 offset:20512\n"
    "  ds_read_b64_tr_b16 v[82:83], v9 offset:23072\n"
    "  ds_read_b64_tr_b16 v[84:85], v9 offset:20544\n"
    "  ds_read_b64_tr_b16 v[86:87], v9 offset:23104\n"
    "  ds_read_b64_tr_b16 v[88:89], v9 offset:20576\n"
    "  ds_read_b64_tr_b16 v[90:91], v9 offset:23136\n"
    "  ds_read_b64_tr_b16 v[92:93], v9 offset:20608\n"
    "  ds_read_b64_tr_b16 v[94:95], v9 offset:23168\n"
    "  s_waitcnt lgkmcnt(0)\n"
    "  s_add_u32 s54, s16, 40960\n"
    "  s_addc_u32 s55, s17, 0\n"
    "  s_mov_b32 m0, 157696\n"
    "  s_nop 0\n"
    "  global_load_lds_dwordx4 v20, s[54:55] offset:0\n"
    "  global_load_lds_dwordx4 v21, s[54:55] offset:1024\n"
    "  global_load_lds_dwordx4 v22, s[54:55] offset:2048\n"
    "  global_load_lds_dwordx4 v23, s[54:55] offset:3072\n"
    "  s_mov_b32 m0, 161792\n"
    "  s_nop 0\n"
    "  global_load_lds_dwordx4 v24, s[54:55] offset:0\n"
    "  s_waitcnt vmcnt(0)\n"
    "  ds_read_b64_tr_b16 v[96:97], v9 offset:20480\n"
    "  ds_read_b64_tr_b16 v[98:99], v9 offset:23040\n"
    "  ds_read_b64_tr_b16 v[100:101], v9 offset:20512\n"
    "  ds_read_b64_tr_b16 v[102:103], v9 offset:23072\n"
    "  ds_read_b64_tr_b16 v[104:105], v9 offset:20544\n"
    "  ds_read_b64_tr_b16 v[106:107], v9 offset:23104\n"
    "  ds_read_b64_tr_b16 v[108:109], v9 offset:20576\n"
    "  ds_read_b64_tr_b16 v[110:111], v9 offset:23136\n"
    "  ds_read_b64_tr_b16 v[112:113], v9 offset:20608\n"
    "  ds_read_b64_tr_b16 v[114:115], v9 offset:23168\n"
    "  s_waitcnt lgkmcnt(0)\n"
    "  s_add_u32 s54, s16, 61440\n"
    "  s_addc_u32 s55, s17, 0\n"
    "  s_mov_b32 m0, 157696\n"
    "  s_nop 0\n"
    "  global_load_lds_dwordx4 v20, s[54:55] offset:0\n"
    "  global_load_lds_dwordx4 v21, s[54:55] offset:1024\n"
    "  global_load_lds_dwordx4 v22, s[54:55] offset:2048\n"
    "  global_load_lds_dwordx4 v23, s[54:55] offset:3072\n"
    "  s_mov_b32 m0, 161792\n"
    "  s_nop 0\n"
    "  global_load_lds_dwordx4 v24, s[54:55] offset:0\n"
    "  s_waitcnt vmcnt(0)\n"
    "  ds_read_b64_tr_b16 v[116:117], v9 offset:20480\n"
    "  ds_read_b64_tr_b16 v[118:119], v9 offset:23040\n"
    "  ds_read_b64_tr_b16 v[120:121], v9 offset:20512\n"
    "  ds_read_b64_tr_b16 v[122:123], v9 offset:23072\n"
    "  ds_read_b64_tr_b16 v[124:125], v9 offset:20544\n"
    "  ds_read_b64_tr_b16 v[126:127], v9 offset:23104\n"
    "  ds_read_b64_tr_b16 v[128:129], v9 offset:20576\n"
    "  ds_read_b64_tr_b16 v[130:131], v9 offset:23136\n"
    "  ds_read_b64_tr_b16 v[132:133], v9 offset:20608\n"
    "  ds_read_b64_tr_b16 v[134:135], v9 offset:23168\n"
    "  s_waitcnt lgkmcnt(0)\n"
    "  s_add_u32 s54, s16, 81920\n"
    "  s_addc_u32 s55, s17, 0\n"
    "  s_mov_b32 m0, 157696\n"
    "  s_nop 0\n"
    "  global_load_lds_dwordx4 v20, s[54:55] offset:0\n"
    "  global_load_lds_dwordx4 v21, s[54:55] offset:1024\n"
    "  global_load_lds_dwordx4 v22, s[54:55] offset:2048\n"
    "  global_load_lds_dwordx4 v23, s[54:55] offset:3072\n"
    "  s_mov_b32 m0, 161792\n"
    "  s_nop 0\n"
    "  global_load_lds_dwordx4 v24, s[54:55] offset:0\n"
    "  s_waitcnt vmcnt(0)\n"
    "  ds_read_b64_tr_b16 v[136:137], v9 offset:20480\n"
    "  ds_read_b64_tr_b16 v[138:139], v9 offset:23040\n"
    "  ds_read_b64_tr_b16 v[140:141], v9 offset:20512\n"
    "  ds_read_b64_tr_b16 v[142:143], v9 offset:23072\n"
    "  ds_read_b64_tr_b16 v[144:145], v9 offset:20544\n"
    "  ds_read_b64_tr_b16 v[146:147], v9 offset:23104\n"
    "  ds_read_b64_tr_b16 v[148:149], v9 offset:20576\n"
    "  ds_read_b64_tr_b16 v[150:151], v9 offset:23136\n"
    "  ds_read_b64_tr_b16 v[152:153], v9 offset:20608\n"
    "  ds_read_b64_tr_b16 v[154:155], v9 offset:23168\n"
    "  s_waitcnt lgkmcnt(0)\n"
    "  s_add_u32 s54, s16, 102400\n"
    "  s_addc_u32 s55, s17, 0\n"
    "  s_mov_b32 m0, 157696\n"
    "  s_nop 0\n"
    "  global_load_lds_dwordx4 v20, s[54:55] offset:0\n"
    "  global_load_lds_dwordx4 v21, s[54:55] offset:1024\n"
    "  global_load_lds_dwordx4 v22, s[54:55] offset:2048\n"
    "  global_load_lds_dwordx4 v23, s[54:55] offset:3072\n"
    "  s_mov_b32 m0, 161792\n"
    "  s_nop 0\n"
    "  global_load_lds_dwordx4 v24, s[54:55] offset:0\n"
    ".Ledm_pl15:\n"
    "  s_sub_u32 s39, s39, 1\n"
    "  s_cbranch_scc1 .Ledm_abort\n"
    "  ds_read_b32 v55, v48 offset:0\n"
    "  s_waitcnt lgkmcnt(0)\n"
    "  v_cmp_gt_u32_e32 vcc, 18, v55\n"
    "  s_cbranch_vccnz .Ledm_pl15\n"
    "  s_add_u32 s54, s16, 122880\n"
    "  s_addc_u32 s55, s17, 0\n"
    "  s_mov_b32 m0, 35840\n"
    "  s_nop 0\n"
    "  global_load_lds_dwordx4 v20, s[54:55] offset:0\n"
    "  global_load_lds_dwordx4 v21, s[54:55] offset:1024\n"
    "  global_load_lds_dwordx4 v22, s[54:55] offset:2048\n"
    "  global_load_lds_dwordx4 v23, s[54:55] offset:3072\n"
    "  s_mov_b32 m0, 39936\n"
    "  s_nop 0\n"
    "  global_load_lds_dwordx4 v24, s[54:55] offset:0\n"
    "  s_waitcnt vmcnt(5)\n"
    "  ds_read_b64_tr_b16 v[156:157], v9 offset:20480\n"
    "  ds_read_b64_tr_b16 v[158:159], v9 offset:23040\n"
    "  ds_read_b64_tr_b16 v[160:161], v9 offset:20512\n"
    "  ds_read_b64_tr_b16 v[162:163], v9 offset:23072\n"
    "  ds_read_b64_tr_b16 v[164:165], v9 offset:20544\n"
    "  ds_read_b64_tr_b16 v[166:167], v9 offset:23104\n"
    "  ds_read_b64_tr_b16 v[168:169], v9 offset:20576\n"
    "  ds_read_b64_tr_b16 v[170:171], v9 offset:23136\n"
    "  ds_read_b64_tr_b16 v[172:173], v9 offset:20608\n"
    "  ds_read_b64_tr_b16 v[174:175], v9 offset:23168\n"
    "  s_waitcnt lgkmcnt(0)\n"
    "  s_add_u32 s54, s16, 143360\n"
    "  s_addc_u32 s55, s17, 0\n"
    "  s_mov_b32 m0, 157696\n"
    "  s_nop 0\n"
    "  global_load_lds_dwordx4 v20, s[54:55] offset:0\n"
    "  global_load_lds_dwordx4 v21, s[54:55] offset:1024\n"
    "  global_load_lds_dwordx4 v22, s[54:55] offset:2048\n"
    "  global_load_lds_dwordx4 v23, s[54:55] offset:3072\n"
    "  s_mov_b32 m0, 161792\n"
    "  s_nop 0\n"
    "  global_load_lds_dwordx4 v24, s[54:55] offset:0\n"
    "  s_waitcnt vmcnt(5)\n"
    "  ds_read_b64_tr_b16 v[176:177], v25 offset:35840\n"
    "  ds_read_b64_tr_b16 v[178:179], v25 offset:38400\n"
    "  ds_read_b64_tr_b16 v[180:181], v25 offset:35872\n"
    "  ds_read_b64_tr_b16 v[182:183], v25 offset:38432\n"
    "  ds_read_b64_tr_b16 v[184:185], v25 offset:35904\n"
    "  ds_read_b64_tr_b16 v[186:187], v25 offset:38464\n"
    "  ds_read_b64_tr_b16 v[188:189], v25 offset:35936\n"
    "  ds_read_b64_tr_b16 v[190:191], v25 offset:38496\n"
    "  ds_read_b64_tr_b16 v[192:193], v25 offset:35968\n"
    "  ds_read_b64_tr_b16 v[194:195], v25 offset:38528\n"
    "  s_waitcnt lgkmcnt(0)\n"
    "  s_add_u32 s54, s16, 163840\n"
    "  s_addc_u32 s55, s17, 0\n"
    "  s_mov_b32 m0, 35840\n"
    "  s_nop 0\n"
    "  global_load_lds_dwordx4 v20, s[54:55] offset:0\n"
    "  global_load_lds_dwordx4 v21, s[54:55] offset:1024\n"
    "  global_load_lds_dwordx4 v22, s[54:55] offset:2048\n"
    "  global_load_lds_dwordx4 v23, s[54:55] offset:3072\n"
    "  s_mov_b32 m0, 39936\n"
    "  s_nop 0\n"
    "  global_load_lds_dwordx4 v24, s[54:55] offset:0\n"
    "  s_waitcnt vmcnt(5)\n"
    "  ds_read_b64_tr_b16 v[196:197], v9 offset:20480\n"
    "  ds_read_b64_tr_b16 v[198:199], v9 offset:23040\n"
    "  ds_read_b64_tr_b16 v[200:201], v9 offset:20512\n"
    "  ds_read_b64_tr_b16 v[202:203], v9 offset:23072\n"
    "  ds_read_b64_tr_b16 v[204:205], v9 offset:20544\n"
    "  ds_read_b64_tr_b16 v[206:207], v9 offset:23104\n"
    "  ds_read_b64_tr_b16 v[208:209], v9 offset:20576\n"
    "  ds_read_b64_tr_b16 v[210:211], v9 offset:23136\n"
    "  ds_read_b64_tr_b16 v[212:213], v9 offset:20608\n"
    "  ds_read_b64_tr_b16 v[214:215], v9 offset:23168\n"
    "  s_waitcnt lgkmcnt(0)\n"
    "  s_add_u32 s54, s16, 184320\n"
    "  s_addc_u32 s55, s17, 0\n"
    "  s_mov_b32 m0, 157696\n"
    "  s_nop 0\n"
    "  global_load_lds_dwordx4 v20, s[54:55] offset:0\n"
    "  global_load_lds_dwordx4 v21, s[54:55] offset:1024\n"
    "  global_load_lds_dwordx4 v22, s[54:55] offset:2048\n"
    "  global_load_lds_dwordx4 v23, s[54:55] offset:3072\n"
    "  s_mov_b32 m0, 161792\n"
    "  s_nop 0\n"
    "  global_load_lds_dwordx4 v24, s[54:55] offset:0\n"
    "  v_add_u32 v254, 35840, v25\n"
    "  v_add_u32 v255, 157696, v25\n"
    "  s_mov_b32 s60, 0\n"
    ".Ledm_G2:\n"
    "  s_setprio 1\n"
    "  ds_read_b64_tr_b16 v[20:21], v53 offset:0\n"
    "  ds_read_b64_tr_b16 v[22:23], v53 offset:4096\n"
    "  ds_read_b64_tr_b16 v[24:25], v53 offset:8192\n"
    "  ds_read_b64_tr_b16 v[26:27], v53 offset:12288\n"
    "  ds_read_b64_tr_b16 v[28:29], v53 offset:16384\n"
    ".Ledm_pl16:\n"
    "  s_sub_u32 s39, s39, 1\n"
    "  s_cbranch_scc1 .Ledm_abort\n"
    "  ds_read_b32 v55, v48 offset:0\n"
    "  s_waitcnt lgkmcnt(0)\n"
    "  v_cmp_gt_u32_e32 vcc, 21, v55\n"
    "  s_cbranch_vccnz .Ledm_pl16\n"
    "  ds_read_b128 v[30:33], v52 offset:40960\n"
    "  ds_read_b128 v[34:37], v52 offset:43008\n"
    "  ds_read_b128 v[38:41], v52 offset:45056\n"
    "  ds_read_b128 v[42:45], v52 offset:47104\n"
    "  s_waitcnt lgkmcnt(3)\n"
    "  v_mfma_f32_16x16x32_bf16 v[0:3], v[30:33], v[56:59], 0\n"
    "  s_waitcnt lgkmcnt(2)\n"
    "  v_mfma_f32_16x16x32_bf16 v[0:3], v[34:37], v[76:79], v[0:3]\n"
    "  v_mfma_f32_16x16x32_bf16 v[4:7], v[30:33], v[60:63], 0\n"
    "  v_mfma_f32_16x16x32_bf16 v[4:7], v[34:37], v[80:83], v[4:7]\n"
    "  v_mfma_f32_16x16x32_bf16 v[8:11], v[30:33], v[64:67], 0\n"
    "  v_mfma_f32_16x16x32_bf16 v[8:11], v[34:37], v[84:87], v[8:11]\n"
    "  v_mfma_f32_16x16x32_bf16 v[12:15], v[30:33], v[68:71], 0\n"
    "  v_mfma_f32_16x16x32_bf16 v[12:15], v[34:37], v[88:91], v[12:15]\n"
    "  v_mfma_f32_16x16x32_bf16 v[16:19], v[30:33], v[72:75], 0\n"
    "  v_mfma_f32_16x16x32_bf16 v[16:19], v[34:37], v[92:95], v[16:19]\n"
    "  ds_read_b128 v[30:33], v52 offset:41984\n"
    "  s_waitcnt lgkmcnt(2)\n"
    "  v_mfma_f32_16x16x32_bf16 v[0:3], v[38:41], v[96:99], v[0:3]\n"
    "  ds_read_b128 v[34:37], v52 offset:44032\n"
    "  v_mfma_f32_16x16x32_bf16 v[4:7], v[38:41], v[100:103], v[4:7]\n"
    "  v_mfma_f32_16x16x32_bf16 v[8:11], v[38:41], v[104:107], v[8:11]\n"
    "  v_mfma_f32_16x16x32_bf16 v[12:15], v[38:41], v[108:111], v[12:15]\n"
    "  v_mfma_f32_16x16x32_bf16 v[16:19], v[38:41], v[112:115], v[16:19]\n"
    "  ds_read_b128 v[38:41], v52 offset:46080\n"
    "  s_waitcnt lgkmcnt(3)\n"
    "  v_mfma_f32_16x16x32_bf16 v[0:3], v[42:45], v[116:119], v[0:3]\n"
    "  v_mfma_f32_16x16x32_bf16 v[4:7], v[42:45], v[120:123], v[4:7]\n"
    "  v_mfma_f32_16x16x32_bf16 v[8:11], v[42:45], v[124:127], v[8:11]\n"
    "  v_mfma_f32_16x16x32_bf16 v[12:15], v[42:45], v[128:131], v[12:15]\n"
    "  v_mfma_f32_16x16x32_bf16 v[16:19], v[42:45], v[132:135], v[16:19]\n"
    "  ds_read_b128 v[42:45], v52 offset:48128\n"
    "  s_waitcnt lgkmcnt(3)\n"
    "  v_mfma_f32_16x16x32_bf16 v[0:3], v[30:33], v[136:139], v[0:3]\n"
    "  v_mfma_f32_16x16x32_bf16 v[4:7], v[30:33], v[140:143], v[4:7]\n"
    "  v_mfma_f32_16x16x32_bf16 v[8:11], v[30:33], v[144:147], v[8:11]\n"
    "  v_mfma_f32_16x16x32_bf16 v[12:15], v[30:33], v[148:151], v[12:15]\n"
    "  v_mfma_f32_16x16x32_bf16 v[16:19], v[30:33], v[152:155], v[16:19]\n"
    "  ds_read_b128 v[30:33], v52 offset:49152\n"
    "  v_mfma_f32_16x16x16_bf16 v[0:3], v[20:21], v[50:51], v[0:3]\n"
    "  v_mfma_f32_16x16x16_bf16 v[4:7], v[22:23], v[50:51], v[4:7]\n"
    "  v_mfma_f32_16x16x16_bf16 v[8:11], v[24:25], v[50:51], v[8:11]\n"
    "  v_mfma_f32_16x16x16_bf16 v[12:15], v[26:27], v[50:51], v[12:15]\n"
    "  v_mfma_f32_16x16x16_bf16 v[16:19], v[28:29], v[50:51], v[16:19]\n"
    "  s_waitcnt lgkmcnt(3)\n"
    "  v_mfma_f32_16x16x32_bf16 v[0:3], v[34:37], v[156:159], v[0:3]\n"
    "  v_mfma_f32_16x16x32_bf16 v[4:7], v[34:37], v[160:163], v[4:7]\n"
    "  v_mfma_f32_16x16x32_bf16 v[8:11], v[34:37], v[164:167], v[8:11]\n"
    "  v_mfma_f32_16x16x32_bf16 v[12:15], v[34:37], v[168:171], v[12:15]\n"
    "  v_mfma_f32_16x16x32_bf16 v[16:19], v[34:37], v[172:175], v[16:19]\n"
    "  ds_read_b128 v[34:37], v52 offset:50176\n"
    "  s_waitcnt lgkmcnt(3)\n"
    "  v_mfma_f32_16x16x32_bf16 v[0:3], v[38:41], v[176:179], v[0:3]\n"
    "  s_waitcnt vmcnt(10)\n"
    "  s_cmp_eq_u32 s60, 0\n"
    "  s_cbranch_scc1 .Ledm_k17\n"
    "  ds_read_b64_tr_b16 v[196:197], v253 offset:0\n"
    "  ds_read_b64_tr_b16 v[198:199], v253 offset:2560\n"
    ".Ledm_k17:\n"
    "  v_mfma_f32_16x16x32_bf16 v[4:7], v[38:41], v[180:183], v[4:7]\n"
    "  s_cmp_eq_u32 s60, 0\n"
    "  s_cbranch_scc1 .Ledm_k18\n"
    "  ds_read_b64_tr_b16 v[200:201], v253 offset:32\n"
    "  ds_read_b64_tr_b16 v[202:203], v253 offset:2592\n"
    ".Ledm_k18:\n"
    "  ds_read_b32 v55, v48 offset:0\n"
    "  v_mfma_f32_16x16x32_bf16 v[8:11], v[38:41], v[184:187], v[8:11]\n"
    "  s_cmp_eq_u32 s60, 0\n"
    "  s_cbranch_scc1 .Ledm_k19\n"
    "  ds_read_b64_tr_b16 v[204:205], v253 offset:64\n"
    "  ds_read_b64_tr_b16 v[206:207], v253 offset:2624\n"
    ".Ledm_k19:\n"
    "  v_mfma_f32_16x16x32_bf16 v[12:15], v[38:41], v[188:191], v[12:15]\n"
    "  s_cmp_eq_u32 s60, 0\n"
    "  s_cbranch_scc1 .Ledm_k20\n"
    "  ds_read_b64_tr_b16 v[208:209], v253 offset:96\n"
    "  ds_read_b64_tr_b16 v[210:211], v253 offset:2656\n"
    "  ds_read_b64_tr_b16 v[212:213], v253 offset:128\n"
    "  ds_read_b64_tr_b16 v[214:215], v253 offset:2688\n"
    ".Ledm_k20:\n"
    "  v_mfma_f32_16x16x32_bf16 v[16:19], v[38:41], v[192:195], v[16:19]\n"
    "  s_waitcnt lgkmcnt(0)\n"
    "  v_cmp_gt_u32_e32 vcc, 22, v55\n"
    "  s_cbranch_vccnz .Ledm_sl21\n"
    ".Ledm_bk22:\n"
    "  ds_read_b128 v[38:41], v52 offset:51200\n"
    "  v_mfma_f32_16x16x32_bf16 v[0:3], v[42:45], v[196:199], v[0:3]\n"
    "  s_waitcnt vmcnt(5)\n"
    "  ds_read_b64_tr_b16 v[216:217], v254 offset:0\n"
    "  ds_read_b64_tr_b16 v[218:219], v254 offset:2560\n"
    "  v_mfma_f32_16x16x32_bf16 v[4:7], v[42:45], v[200:203], v[4:7]\n"
    "  ds_read_b64_tr_b16 v[220:221], v254 offset:32\n"
    "  ds_read_b64_tr_b16 v[222:223], v254 offset:2592\n"
    "  s_waitcnt lgkmcnt(5)\n"
    "  v_mfma_f32_16x16x32_bf16 v[8:11], v[42:45], v[204:207], v[8:11]\n"
    "  ds_read_b64_tr_b16 v[224:225], v254 offset:64\n"
    "  ds_read_b64_tr_b16 v[226:227], v254 offset:2624\n"
    "  s_waitcnt lgkmcnt(7)\n"
    "  v_mfma_f32_16x16x32_bf16 v[12:15], v[42:45], v[208:211], v[12:15]\n"
    "  ds_read_b64_tr_b16 v[228:229], v254 offset:96\n"
    "  ds_read_b64_tr_b16 v[230:231], v254 offset:2656\n"
    "  ds_read_b64_tr_b16 v[232:233], v254 offset:128\n"
    "  ds_read_b64_tr_b16 v[234:235], v254 offset:2688\n"
    "  s_waitcnt lgkmcnt(11)\n"
    "  v_mfma_f32_16x16x32_bf16 v[16:19], v[42:45], v[212:215], v[16:19]\n"
    "  ds_read_b128 v[42:45], v52 offset:53248\n"
    "  s_waitcnt lgkmcnt(9)\n"
    "  v_mfma_f32_16x16x32_bf16 v[0:3], v[30:33], v[216:219], v[0:3]\n"
    "  s_waitcnt vmcnt(0)\n"
    "  ds_read_b64_tr_b16 v[236:237], v255 offset:0\n"
    "  ds_read_b64_tr_b16 v[238:239], v255 offset:2560\n"
    "  s_waitcnt lgkmcnt(9)\n"
    "  v_mfma_f32_16x16x32_bf16 v[4:7], v[30:33], v[220:223], v[4:7]\n"
    "  ds_read_b64_tr_b16 v[240:241], v255 offset:32\n"
    "  ds_read_b64_tr_b16 v[242:243], v255 offset:2592\n"
    "  s_waitcnt lgkmcnt(9)\n"
    "  v_mfma_f32_16x16x32_bf16 v[8:11], v[30:33], v[224:227], v[8:11]\n"
    "  ds_read_b64_tr_b16 v[244:245], v255 offset:64\n"
    "  ds_read_b64_tr_b16 v[246:247], v255 offset:2624\n"
    "  s_waitcnt lgkmcnt(9)\n"
    "  v_mfma_f32_16x16x32_bf16 v[12:15], v[30:33], v[228:231], v[12:15]\n"
    "  ds_read_b64_tr_b16 v[248:249], v255 offset:96\n"
    "  ds_read_b64_tr_b16 v[250:251], v255 offset:2656\n"
    "  ds_read_b64_tr_b16 v[252:253], v255 offset:128\n"
    "  ds_read_b64_tr_b16 v[254:255], v255 offset:2688\n"
    "  s_waitcnt lgkmcnt(11)\n"
    "  v_mfma_f32_16x16x32_bf16 v[16:19], v[30:33], v[232:235], v[16:19]\n"
    "  v_mov_b32 v55, 2\n"
    "  ds_write_b32 v49, v55 offset:1024\n"
    "  s_waitcnt lgkmcnt(9)\n"
    "  v_mfma_f32_16x16x32_bf16 v[0:3], v[34:37], v[236:239], v[0:3]\n"
    "  s_waitcnt lgkmcnt(7)\n"
    "  v_mfma_f32_16x16x32_bf16 v[4:7], v[34:37], v[240:243], v[4:7]\n"
    "  s_waitcnt lgkmcnt(5)\n"
    "  v_mfma_f32_16x16x32_bf16 v[8:11], v[34:37], v[244:247], v[8:11]\n"
    "  v_cvt_pk_bf16_f32 v0, v0, v1\n"
    "  v_cvt_pk_bf16_f32 v1, v2, v3\n"
    "  s_waitcnt lgkmcnt(3)\n"
    "  v_mfma_f32_16x16x32_bf16 v[12:15], v[34:37], v[248:251], v[12:15]\n"
    "  ds_write_b64 v46, v[0:1] offset:0\n"
    "  v_cvt_pk_bf16_f32 v4, v4, v5\n"
    "  v_cvt_pk_bf16_f32 v5, v6, v7\n"
    "  s_waitcnt lgkmcnt(2)\n"
    "  v_mfma_f32_16x16x32_bf16 v[16:19], v[34:37], v[252:255], v[16:19]\n"
    "  ds_write_b64 v46, v[4:5] offset:4096\n"
    "  v_cvt_pk_bf16_f32 v8, v8, v9\n"
    "  v_cvt_pk_bf16_f32 v9, v10, v11\n"
    "  v_mfma_f32_16x16x32_bf16 v[0:3], v[38:41], v[56:59], 0\n"
    "  ds_write_b64 v46, v[8:9] offset:8192\n"
    "  v_cvt_pk_bf16_f32 v12, v12, v13\n"
    "  v_cvt_pk_bf16_f32 v13, v14, v15\n"
    "  ds_read_b128 v[30:33], v52 offset:55296\n"
    "  v_mfma_f32_16x16x32_bf16 v[0:3], v[42:45], v[76:79], v[0:3]\n"
    "  ds_write_b64 v46, v[12:13] offset:12288\n"
    "  v_cvt_pk_bf16_f32 v16, v16, v17\n"
    "  v_cvt_pk_bf16_f32 v17, v18, v19\n"
    "  v_mfma_f32_16x16x32_bf16 v[4:7], v[38:41], v[60:63], 0\n"
    "  ds_write_b64 v46, v[16:17] offset:16384\n"
    "  v_mfma_f32_16x16x32_bf16 v[4:7], v[42:45], v[80:83], v[4:7]\n"
    "  ds_read_b128 v[34:37], v52 offset:57344\n"
    "  v_mfma_f32_16x16x32_bf16 v[8:11], v[38:41], v[64:67], 0\n"
    "  v_mfma_f32_16x16x32_bf16 v[8:11], v[42:45], v[84:87], v[8:11]\n"
    "  v_mfma_f32_16x16x32_bf16 v[12:15], v[38:41], v[68:71], 0\n"
    "  v_mfma_f32_16x16x32_bf16 v[12:15], v[42:45], v[88:91], v[12:15]\n"
    "  v_mfma_f32_16x16x32_bf16 v[16:19], v[38:41], v[72:75], 0\n"
    "  v_xor_b32 v53, 32, v53\n"
    "  v_xor_b32 v46, 32, v46\n"
    "  v_mfma_f32_16x16x32_bf16 v[16:19], v[42:45], v[92:95], v[16:19]\n"
    "  ds_read_b128 v[38:41], v52 offset:52224\n"
    "  s_waitcnt lgkmcnt(4)\n"
    "  v_mfma_f32_16x16x32_bf16 v[0:3], v[30:33], v[96:99], v[0:3]\n"
    "  ds_read_b128 v[42:45], v52 offset:54272\n"
    "  v_mfma_f32_16x16x32_bf16 v[4:7], v[30:33], v[100:103], v[4:7]\n"
    "  ds_read_b64_tr_b16 v[20:21], v53 offset:0\n"
    "  v_mfma_f32_16x16x32_bf16 v[8:11], v[30:33], v[104:107], v[8:11]\n"
    "  ds_read_b64_tr_b16 v[22:23], v53 offset:4096\n"
    "  v_mfma_f32_16x16x32_bf16 v[12:15], v[30:33], v[108:111], v[12:15]\n"
    "  ds_read_b64_tr_b16 v[24:25], v53 offset:8192\n"
    "  v_mfma_f32_16x16x32_bf16 v[16:19], v[30:33], v[112:115], v[16:19]\n"
    "  ds_read_b128 v[30:33], v52 offset:56320\n"
    "  s_waitcnt lgkmcnt(6)\n"
    "  v_mfma_f32_16x16x32_bf16 v[0:3], v[34:37], v[116:119], v[0:3]\n"
    "  ds_read_b64_tr_b16 v[26:27], v53 offset:12288\n"
    "  v_mfma_f32_16x16x32_bf16 v[4:7], v[34:37], v[120:123], v[4:7]\n"
    "  v_add_u32 v49, 61440, v52\n"
    "  ds_read_b64_tr_b16 v[28:29], v53 offset:16384\n"
    "  v_mfma_f32_16x16x32_bf16 v[8:11], v[34:37], v[124:127], v[8:11]\n"
    "  v_mfma_f32_16x16x32_bf16 v[12:15], v[34:37], v[128:131], v[12:15]\n"
    "  v_mfma_f32_16x16x32_bf16 v[16:19], v[34:37], v[132:135], v[16:19]\n"
    "  ds_read_b128 v[34:37], v52 offset:58368\n"
    "  s_waitcnt lgkmcnt(8)\n"
    "  v_mfma_f32_16x16x32_bf16 v[0:3], v[38:41], v[136:139], v[0:3]\n"
    "  v_mfma_f32_16x16x32_bf16 v[4:7], v[38:41], v[140:143], v[4:7]\n"
    "  v_mfma_f32_16x16x32_bf16 v[8:11], v[38:41], v[144:147], v[8:11]\n"
    "  v_mfma_f32_16x16x32_bf16 v[12:15], v[38:41], v[148:151], v[12:15]\n"
    "  v_mfma_f32_16x16x32_bf16 v[16:19], v[38:41], v[152:155], v[16:19]\n"
    "  ds_read_b128 v[38:41], v52 offset:59392\n"
    "  s_waitcnt lgkmcnt(7)\n"
    "  v_mfma_f32_16x16x16_bf16 v[0:3], v[20:21], v[50:51], v[0:3]\n"
    "  s_waitcnt lgkmcnt(6)\n"
    "  v_mfma_f32_16x16x16_bf16 v[4:7], v[22:23], v[50:51], v[4:7]\n"
    "  s_waitcnt lgkmcnt(5)\n"
    "  v_mfma_f32_16x16x16_bf16 v[8:11], v[24:25], v[50:51], v[8:11]\n"
    "  s_waitcnt lgkmcnt(3)\n"
    "  v_mfma_f32_16x16x16_bf16 v[12:15], v[26:27], v[50:51], v[12:15]\n"
    "  s_waitcnt lgkmcnt(2)\n"
    "  v_mfma_f32_16x16x16_bf16 v[16:19], v[28:29], v[50:51], v[16:19]\n"
    "  v_mfma_f32_16x16x32_bf16 v[0:3], v[42:45], v[156:159], v[0:3]\n"
    "  v_mfma_f32_16x16x32_bf16 v[4:7], v[42:45], v[160:163], v[4:7]\n"
    "  v_mfma_f32_16x16x32_bf16 v[8:11], v[42:45], v[164:167], v[8:11]\n"
    "  v_mfma_f32_16x16x32_bf16 v[12:15], v[42:45], v[168:171], v[12:15]\n"
    "  v_mfma_f32_16x16x32_bf16 v[16:19], v[42:45], v[172:175], v[16:19]\n"
    "  ds_read_b128 v[42:45], v52 offset:60416\n"
    "  v_mfma_f32_16x16x32_bf16 v[0:3], v[30:33], v[176:179], v[0:3]\n"
    "  v_mfma_f32_16x16x32_bf16 v[4:7], v[30:33], v[180:183], v[4:7]\n"
    "  v_mfma_f32_16x16x32_bf16 v[8:11], v[30:33], v[184:187], v[8:11]\n"
    "  v_mfma_f32_16x16x32_bf16 v[12:15], v[30:33], v[188:191], v[12:15]\n"
    "  v_mfma_f32_16x16x32_bf16 v[16:19], v[30:33], v[192:195], v[16:19]\n"
    "  s_waitcnt lgkmcnt(2)\n"
    "  v_mfma_f32_16x16x32_bf16 v[0:3], v[34:37], v[196:199], v[0:3]\n"
    "  v_mfma_f32_16x16x32_bf16 v[4:7], v[34:37], v[200:203], v[4:7]\n"
    "  v_mfma_f32_16x16x32_bf16 v[8:11], v[34:37], v[204:207], v[8:11]\n"
    "  v_mfma_f32_16x16x32_bf16 v[12:15], v[34:37], v[208:211], v[12:15]\n"
    "  v_mfma_f32_16x16x32_bf16 v[16:19], v[34:37], v[212:215], v[16:19]\n"
    "  s_waitcnt lgkmcnt(1)\n"
    "  v_mfma_f32_16x16x32_bf16 v[0:3], v[38:41], v[216:219], v[0:3]\n"
    "  s_waitcnt lgkmcnt(0)\n"
    "  v_mfma_f32_16x16x32_bf16 v[0:3], v[42:45], v[236:239], v[0:3]\n"
    "  ds_read_b32 v55, v48 offset:0\n"
    "  v_mfma_f32_16x16x32_bf16 v[4:7], v[38:41], v[220:223], v[4:7]\n"
    "  v_mfma_f32_16x16x32_bf16 v[4:7], v[42:45], v[240:243], v[4:7]\n"
    "  v_cvt_pk_bf16_f32 v0, v0, v1\n"
    "  v_cvt_pk_bf16_f32 v1, v2, v3\n"
    "  v_mfma_f32_16x16x32_bf16 v[8:11], v[38:41], v[224:227], v[8:11]\n"
    "  s_waitcnt lgkmcnt(0)\n"
    "  v_cmp_gt_u32_e32 vcc, 23, v55\n"
    "  s_cbranch_vccnz .Ledm_sl23\n"
    ".Ledm_bk24:\n"
    "  ds_read_b128 v[30:33], v49 offset:0\n"
    "  ds_write_b64 v46, v[0:1] offset:0\n"
    "  v_mfma_f32_16x16x32_bf16 v[8:11], v[42:45], v[244:247], v[8:11]\n"
    "  ds_read_b128 v[34:37], v49 offset:2048\n"
    "  v_cvt_pk_bf16_f32 v4, v4, v5\n"
    "  v_cvt_pk_bf16_f32 v5, v6, v7\n"
    "  v_mfma_f32_16x16x32_bf16 v[12:15], v[38:41], v[228:231], v[12:15]\n"
    "  ds_write_b64 v46, v[4:5] offset:4096\n"
    "  v_mfma_f32_16x16x32_bf16 v[12:15], v[42:45], v[248:251], v[12:15]\n"
    "  v_cvt_pk_bf16_f32 v8, v8, v9\n"
    "  v_cvt_pk_bf16_f32 v9, v10, v11\n"
    "  v_mfma_f32_16x16x32_bf16 v[16:19], v[38:41], v[232:235], v[16:19]\n"
    "  ds_write_b64 v46, v[8:9] offset:8192\n"
    "  v_mfma_f32_16x16x32_bf16 v[16:19], v[42:45], v[252:255], v[16:19]\n"
    "  v_cvt_pk_bf16_f32 v12, v12, v13\n"
    "  v_cvt_pk_bf16_f32 v13, v14, v15\n"
    "  s_waitcnt lgkmcnt(4)\n"
    "  v_mfma_f32_16x16x32_bf16 v[0:3], v[30:33], v[56:59], 0\n"
    "  ds_write_b64 v46, v[12:13] offset:12288\n"
    "  ds_read_b128 v[38:41], v49 offset:4096\n"
    "  s_waitcnt lgkmcnt(4)\n"
    "  v_mfma_f32_16x16x32_bf16 v[0:3], v[34:37], v[76:79], v[0:3]\n"
    "  v_cvt_pk_bf16_f32 v16, v16, v17\n"
    "  v_cvt_pk_bf16_f32 v17, v18, v19\n"
    "  ds_read2st64_b64 v[20:23], v47 offset0:0 offset1:2\n"
    "  v_mfma_f32_16x16x32_bf16 v[4:7], v[30:33], v[60:63], 0\n"
    "  ds_write_b64 v46, v[16:17] offset:16384\n"
    "  v_mfma_f32_16x16x32_bf16 v[4:7], v[34:37], v[80:83], v[4:7]\n"
    "  ds_read_b128 v[42:45], v49 offset:6144\n"
    "  v_mfma_f32_16x16x32_bf16 v[8:11], v[30:33], v[64:67], 0\n"
    "  v_mfma_f32_16x16x32_bf16 v[8:11], v[34:37], v[84:87], v[8:11]\n"
    "  v_mfma_f32_16x16x32_bf16 v[12:15], v[30:33], v[68:71], 0\n"
    "  s_waitcnt lgkmcnt(2)\n"
    "  global_store_dwordx4 v54, v[20:23], s[40:41] offset:0 sc1 nt\n"
    "  v_mfma_f32_16x16x32_bf16 v[12:15], v[34:37], v[88:91], v[12:15]\n"
    "  v_mfma_f32_16x16x32_bf16 v[16:19], v[30:33], v[72:75], 0\n"
    "  v_xor_b32 v53, 96, v53\n"
    "  v_xor_b32 v46, 96, v46\n"
    "  v_mfma_f32_16x16x32_bf16 v[16:19], v[34:37], v[92:95], v[16:19]\n"
    "  ds_read_b128 v[30:33], v49 offset:1024\n"
    "  v_mfma_f32_16x16x32_bf16 v[0:3], v[38:41], v[96:99], v[0:3]\n"
    "  ds_read_b128 v[34:37], v49 offset:3072\n"
    "  v_mfma_f32_16x16x32_bf16 v[4:7], v[38:41], v[100:103], v[4:7]\n"
    "  ds_read_b64_tr_b16 v[20:21], v53 offset:0\n"
    "  v_mfma_f32_16x16x32_bf16 v[8:11], v[38:41], v[104:107], v[8:11]\n"
    "  ds_read_b64_tr_b16 v[22:23], v53 offset:4096\n"
    "  v_mfma_f32_16x16x32_bf16 v[12:15], v[38:41], v[108:111], v[12:15]\n"
    "  ds_read_b64_tr_b16 v[24:25], v53 offset:8192\n"
    "  v_mfma_f32_16x16x32_bf16 v[16:19], v[38:41], v[112:115], v[16:19]\n"
    "  ds_read_b128 v[38:41], v49 offset:5120\n"
    "  s_waitcnt lgkmcnt(6)\n"
    "  v_mfma_f32_16x16x32_bf16 v[0:3], v[42:45], v[116:119], v[0:3]\n"
    "  ds_read_b64_tr_b16 v[26:27], v53 offset:12288\n"
    "  v_mfma_f32_16x16x32_bf16 v[4:7], v[42:45], v[120:123], v[4:7]\n"
    "  ds_read_b64_tr_b16 v[28:29], v53 offset:16384\n"
    "  v_mfma_f32_16x16x32_bf16 v[8:11], v[42:45], v[124:127], v[8:11]\n"
    "  v_mfma_f32_16x16x32_bf16 v[12:15], v[42:45], v[128:131], v[12:15]\n"
    "  v_mfma_f32_16x16x32_bf16 v[16:19], v[42:45], v[132:135], v[16:19]\n"
    "  ds_read_b128 v[42:45], v49 offset:7168\n"
    "  s_waitcnt lgkmcnt(8)\n"
    "  v_mfma_f32_16x16x32_bf16 v[0:3], v[30:33], v[136:139], v[0:3]\n"
    "  v_mfma_f32_16x16x32_bf16 v[4:7], v[30:33], v[140:143], v[4:7]\n"
    "  v_mfma_f32_16x16x32_bf16 v[8:11], v[30:33], v[144:147], v[8:11]\n"
    "  v_mfma_f32_16x16x32_bf16 v[12:15], v[30:33], v[148:151], v[12:15]\n"
    "  v_mfma_f32_16x16x32_bf16 v[16:19], v[30:33], v[152:155], v[16:19]\n"
    "  ds_read_b128 v[30:33], v49 offset:8192\n"
    "  s_waitcnt lgkmcnt(7)\n"
    "  v_mfma_f32_16x16x16_bf16 v[0:3], v[20:21], v[50:51], v[0:3]\n"
    "  s_waitcnt lgkmcnt(6)\n"
    "  v_mfma_f32_16x16x16_bf16 v[4:7], v[22:23], v[50:51], v[4:7]\n"
    "  s_waitcnt lgkmcnt(5)\n"
    "  v_mfma_f32_16x16x16_bf16 v[8:11], v[24:25], v[50:51], v[8:11]\n"
    "  s_waitcnt lgkmcnt(3)\n"
    "  v_mfma_f32_16x16x16_bf16 v[12:15], v[26:27], v[50:51], v[12:15]\n"
    "  s_waitcnt lgkmcnt(2)\n"
    "  v_mfma_f32_16x16x16_bf16 v[16:19], v[28:29], v[50:51], v[16:19]\n"
    "  v_mfma_f32_16x16x32_bf16 v[0:3], v[34:37], v[156:159], v[0:3]\n"
    "  ds_read2st64_b64 v[24:27], v47 offset0:8 offset1:10\n"
    "  v_mfma_f32_16x16x32_bf16 v[4:7], v[34:37], v[160:163], v[4:7]\n"
    "  v_mfma_f32_16x16x32_bf16 v[8:11], v[34:37], v[164:167], v[8:11]\n"
    "  v_mfma_f32_16x16x32_bf16 v[12:15], v[34:37], v[168:171], v[12:15]\n"
    "  v_mfma_f32_16x16x32_bf16 v[16:19], v[34:37], v[172:175], v[16:19]\n"
    "  ds_read_b128 v[34:37], v49 offset:9216\n"
    "  v_mfma_f32_16x16x32_bf16 v[0:3], v[38:41], v[176:179], v[0:3]\n"
    "  s_waitcnt lgkmcnt(1)\n"
    "  global_store_dwordx4 v54, v[24:27], s[42:43] offset:0 sc1 nt\n"
    "  v_mfma_f32_16x16x32_bf16 v[4:7], v[38:41], v[180:183], v[4:7]\n"
    "  v_mfma_f32_16x16x32_bf16 v[8:11], v[38:41], v[184:187], v[8:11]\n"
    "  v_mfma_f32_16x16x32_bf16 v[12:15], v[38:41], v[188:191], v[12:15]\n"
    "  v_mfma_f32_16x16x32_bf16 v[16:19], v[38:41], v[192:195], v[16:19]\n"
    "  v_mfma_f32_16x16x32_bf16 v[0:3], v[42:45], v[196:199], v[0:3]\n"
    "  v_mfma_f32_16x16x32_bf16 v[4:7], v[42:45], v[200:203], v[4:7]\n"
    "  v_mfma_f32_16x16x32_bf16 v[8:11], v[42:45], v[204:207], v[8:11]\n"
    "  v_mfma_f32_16x16x32_bf16 v[12:15], v[42:45], v[208:211], v[12:15]\n"
    "  v_mfma_f32_16x16x32_bf16 v[16:19], v[42:45], v[212:215], v[16:19]\n"
    "  v_mfma_f32_16x16x32_bf16 v[0:3], v[30:33], v[216:219], v[0:3]\n"
    "  s_waitcnt lgkmcnt(0)\n"
    "  v_mfma_f32_16x16x32_bf16 v[0:3], v[34:37], v[236:239], v[0:3]\n"
    "  ds_read_b32 v55, v48 offset:0\n"
    "  v_mfma_f32_16x16x32_bf16 v[4:7], v[30:33], v[220:223], v[4:7]\n"
    "  ds_read2st64_b64 v[20:23], v47 offset0:16 offset1:18\n"
    "  v_mfma_f32_16x16x32_bf16 v[4:7], v[34:37], v[240:243], v[4:7]\n"
    "  v_cvt_pk_bf16_f32 v0, v0, v1\n"
    "  v_cvt_pk_bf16_f32 v1, v2, v3\n"
    "  v_mfma_f32_16x16x32_bf16 v[8:11], v[30:33], v[224:227], v[8:11]\n"
    "  s_waitcnt lgkmcnt(1)\n"
    "  v_cmp_gt_u32_e32 vcc, 24, v55\n"
    "  s_cbranch_vccnz .Ledm_sl25\n"
    ".Ledm_bk26:\n"
    "  ds_read_b128 v[38:41], v52 offset:0\n"
    "  ds_write_b64 v46, v[0:1] offset:0\n"
    "  v_mfma_f32_16x16x32_bf16 v[8:11], v[34:37], v[244:247], v[8:11]\n"
    "  ds_read_b128 v[42:45], v52 offset:2048\n"
    "  v_cvt_pk_bf16_f32 v4, v4, v5\n"
    "  v_cvt_pk_bf16_f32 v5, v6, v7\n"
    "  v_mfma_f32_16x16x32_bf16 v[12:15], v[30:33], v[228:231], v[12:15]\n"
    "  ds_write_b64 v46, v[4:5] offset:4096\n"
    "  v_mfma_f32_16x16x32_bf16 v[12:15], v[34:37], v[248:251], v[12:15]\n"
    "  s_waitcnt lgkmcnt(4)\n"
    "  global_store_dwordx4 v54, v[20:23], s[44:45] offset:0 sc1 nt\n"
    "  v_cvt_pk_bf16_f32 v8, v8, v9\n"
    "  v_cvt_pk_bf16_f32 v9, v10, v11\n"
    "  v_mfma_f32_16x16x32_bf16 v[16:19], v[30:33], v[232:235], v[16:19]\n"
    "  ds_write_b64 v46, v[8:9] offset:8192\n"
    "  v_mfma_f32_16x16x32_bf16 v[16:19], v[34:37], v[252:255], v[16:19]\n"
    "  v_cvt_pk_bf16_f32 v12, v12, v13\n"
    "  v_cvt_pk_bf16_f32 v13, v14, v15\n"
    "  s_waitcnt lgkmcnt(4)\n"
    "  v_mfma_f32_16x16x32_bf16 v[0:3], v[38:41], v[56:59], 0\n"
    "  ds_write_b64 v46, v[12:13] offset:12288\n"
    "  ds_read_b128 v[30:33], v52 offset:4096\n"
    "  s_waitcnt lgkmcnt(4)\n"
    "  v_mfma_f32_16x16x32_bf16 v[0:3], v[42:45], v[76:79], v[0:3]\n"
    "  v_cvt_pk_bf16_f32 v16, v16, v17\n"
    "  v_cvt_pk_bf16_f32 v17, v18, v19\n"
    "  v_mfma_f32_16x16x32_bf16 v[4:7], v[38:41], v[60:63], 0\n"
    "  ds_write_b64 v46, v[16:17] offset:16384\n"
    "  v_mfma_f32_16x16x32_bf16 v[4:7], v[42:45], v[80:83], v[4:7]\n"
    "  ds_read_b128 v[34:37], v52 offset:6144\n"
    "  v_mfma_f32_16x16x32_bf16 v[8:11], v[38:41], v[64:67], 0\n"
    "  v_mfma_f32_16x16x32_bf16 v[8:11], v[42:45], v[84:87], v[8:11]\n"
    "  ds_read2st64_b64 v[24:27], v47 offset0:24 offset1:26\n"
    "  v_mfma_f32_16x16x32_bf16 v[12:15], v[38:41], v[68:71], 0\n"
    "  v_mfma_f32_16x16x32_bf16 v[12:15], v[42:45], v[88:91], v[12:15]\n"
    "  v_mfma_f32_16x16x32_bf16 v[16:19], v[38:41], v[72:75], 0\n"
    "  v_xor_b32 v53, 32, v53\n"
    "  v_xor_b32 v46, 32, v46\n"
    "  v_mfma_f32_16x16x32_bf16 v[16:19], v[42:45], v[92:95], v[16:19]\n"
    "  ds_read_b128 v[38:41], v52 offset:1024\n"
    "  s_waitcnt lgkmcnt(4)\n"
    "  v_mfma_f32_16x16x32_bf16 v[0:3], v[30:33], v[96:99], v[0:3]\n"
    "  s_waitcnt lgkmcnt(1)\n"
    "  global_store_dwordx4 v54, v[24:27], s[46:47] offset:0 sc1 nt\n"
    "  ds_read_b128 v[42:45], v52 offset:3072\n"
    "  v_mfma_f32_16x16x32_bf16 v[4:7], v[30:33], v[100:103], v[4:7]\n"
    "  ds_read_b64_tr_b16 v[20:21], v53 offset:0\n"
    "  v_mfma_f32_16x16x32_bf16 v[8:11], v[30:33], v[104:107], v[8:11]\n"
    "  ds_read_b64_tr_b16 v[22:23], v53 offset:4096\n"
    "  v_mfma_f32_16x16x32_bf16 v[12:15], v[30:33], v[108:111], v[12:15]\n"
    "  ds_read_b64_tr_b16 v[24:25], v53 offset:8192\n"
    "  v_mfma_f32_16x16x32_bf16 v[16:19], v[30:33], v[112:115], v[16:19]\n"
    "  ds_read_b128 v[30:33], v52 offset:5120\n"
    "  v_mfma_f32_16x16x32_bf16 v[0:3], v[34:37], v[116:119], v[0:3]\n"
    "  ds_read_b64_tr_b16 v[26:27], v53 offset:12288\n"
    "  v_mfma_f32_16x16x32_bf16 v[4:7], v[34:37], v[120:123], v[4:7]\n"
    "  ds_read_b64_tr_b16 v[28:29], v53 offset:16384\n"
    "  v_mfma_f32_16x16x32_bf16 v[8:11], v[34:37], v[124:127], v[8:11]\n"
    "  v_mfma_f32_16x16x32_bf16 v[12:15], v[34:37], v[128:131], v[12:15]\n"
    "  v_mfma_f32_16x16x32_bf16 v[16:19], v[34:37], v[132:135], v[16:19]\n"
    "  ds_read_b128 v[34:37], v52 offset:7168\n"
    "  s_waitcnt lgkmcnt(8)\n"
    "  v_mfma_f32_16x16x32_bf16 v[0:3], v[38:41], v[136:139], v[0:3]\n"
    "  v_mfma_f32_16x16x32_bf16 v[4:7], v[38:41], v[140:143], v[4:7]\n"
    "  v_mfma_f32_16x16x32_bf16 v[8:11], v[38:41], v[144:147], v[8:11]\n"
    "  v_mfma_f32_16x16x32_bf16 v[12:15], v[38:41], v[148:151], v[12:15]\n"
    "  v_mfma_f32_16x16x32_bf16 v[16:19], v[38:41], v[152:155], v[16:19]\n"
    "  ds_read_b128 v[38:41], v52 offset:8192\n"
    "  s_waitcnt lgkmcnt(7)\n"
    "  v_mfma_f32_16x16x16_bf16 v[0:3], v[20:21], v[50:51], v[0:3]\n"
    "  s_waitcnt lgkmcnt(6)\n"
    "  v_mfma_f32_16x16x16_bf16 v[4:7], v[22:23], v[50:51], v[4:7]\n"
    "  s_waitcnt lgkmcnt(5)\n"
    "  v_mfma_f32_16x16x16_bf16 v[8:11], v[24:25], v[50:51], v[8:11]\n"
    "  s_waitcnt lgkmcnt(3)\n"
    "  v_mfma_f32_16x16x16_bf16 v[12:15], v[26:27], v[50:51], v[12:15]\n"
    "  s_waitcnt lgkmcnt(2)\n"
    "  v_mfma_f32_16x16x16_bf16 v[16:19], v[28:29], v[50:51], v[16:19]\n"
    "  v_mfma_f32_16x16x32_bf16 v[0:3], v[42:45], v[156:159], v[0:3]\n"
    "  v_mfma_f32_16x16x32_bf16 v[4:7], v[42:45], v[160:163], v[4:7]\n"
    "  v_mfma_f32_16x16x32_bf16 v[8:11], v[42:45], v[164:167], v[8:11]\n"
    "  ds_read2st64_b64 v[20:23], v47 offset0:32 offset1:34\n"
    "  v_mfma_f32_16x16x32_bf16 v[12:15], v[42:45], v[168:171], v[12:15]\n"
    "  v_xor_b32 v47, 64, v47\n"
    "  v_mfma_f32_16x16x32_bf16 v[16:19], v[42:45], v[172:175], v[16:19]\n"
    "  ds_read_b128 v[42:45], v52 offset:9216\n"
    "  v_mfma_f32_16x16x32_bf16 v[0:3], v[30:33], v[176:179], v[0:3]\n"
    "  v_mfma_f32_16x16x32_bf16 v[4:7], v[30:33], v[180:183], v[4:7]\n"
    "  v_mfma_f32_16x16x32_bf16 v[8:11], v[30:33], v[184:187], v[8:11]\n"
    "  s_waitcnt lgkmcnt(1)\n"
    "  global_store_dwordx4 v54, v[20:23], s[48:49] offset:0 sc1 nt\n"
    "  v_mfma_f32_16x16x32_bf16 v[12:15], v[30:33], v[188:191], v[12:15]\n"
    "  v_mfma_f32_16x16x32_bf16 v[16:19], v[30:33], v[192:195], v[16:19]\n"
    "  v_mfma_f32_16x16x32_bf16 v[0:3], v[34:37], v[196:199], v[0:3]\n"
    "  v_mfma_f32_16x16x32_bf16 v[4:7], v[34:37], v[200:203], v[4:7]\n"
    "  v_mfma_f32_16x16x32_bf16 v[8:11], v[34:37], v[204:207], v[8:11]\n"
    "  v_mfma_f32_16x16x32_bf16 v[12:15], v[34:37], v[208:211], v[12:15]\n"
    "  v_mfma_f32_16x16x32_bf16 v[16:19], v[34:37], v[212:215], v[16:19]\n"
    "  v_mfma_f32_16x16x32_bf16 v[0:3], v[38:41], v[216:219], v[0:3]\n"
    "  s_waitcnt lgkmcnt(0)\n"
    "  v_mfma_f32_16x16x32_bf16 v[0:3], v[42:45], v[236:239], v[0:3]\n"
    "  ds_read_b32 v55, v48 offset:0\n"
    "  v_mfma_f32_16x16x32_bf16 v[4:7], v[38:41], v[220:223], v[4:7]\n"
    "  v_mfma_f32_16x16x32_bf16 v[4:7], v[42:45], v[240:243], v[4:7]\n"
    "  v_cvt_pk_bf16_f32 v0, v0, v1\n"
    "  v_cvt_pk_bf16_f32 v1, v2, v3\n"
    "  v_mfma_f32_16x16x32_bf16 v[8:11], v[38:41], v[224:227], v[8:11]\n"
    "  s_waitcnt lgkmcnt(0)\n"
    "  v_cmp_gt_u32_e32 vcc, 25, v55\n"
    "  s_cbranch_vccnz .Ledm_sl27\n"
    ".Ledm_bk28:\n"
    "  ds_read_b128 v[30:33], v52 offset:10240\n"
    "  ds_write_b64 v46, v[0:1] offset:0\n"
    "  v_mfma_f32_16x16x32_bf16 v[8:11], v[42:45], v[244:247], v[8:11]\n"
    "  ds_read_b128 v[34:37], v52 offset:12288\n"
    "  v_cvt_pk_bf16_f32 v4, v4, v5\n"
    "  v_cvt_pk_bf16_f32 v5, v6, v7\n"
    "  v_mfma_f32_16x16x32_bf16 v[12:15], v[38:41], v[228:231], v[12:15]\n"
    "  ds_write_b64 v46, v[4:5] offset:4096\n"
    "  v_mfma_f32_16x16x32_bf16 v[12:15], v[42:45], v[248:251], v[12:15]\n"
    "  v_cvt_pk_bf16_f32 v8, v8, v9\n"
    "  v_cvt_pk_bf16_f32 v9, v10, v11\n"
    "  v_mfma_f32_16x16x32_bf16 v[16:19], v[38:41], v[232:235], v[16:19]\n"
    "  ds_write_b64 v46, v[8:9] offset:8192\n"
    "  v_mfma_f32_16x16x32_bf16 v[16:19], v[42:45], v[252:255], v[16:19]\n"
    "  v_cvt_pk_bf16_f32 v12, v12, v13\n"
    "  v_cvt_pk_bf16_f32 v13, v14, v15\n"
    "  s_waitcnt lgkmcnt(4)\n"
    "  v_mfma_f32_16x16x32_bf16 v[0:3], v[30:33], v[56:59], 0\n"
    "  ds_write_b64 v46, v[12:13] offset:12288\n"
    "  ds_read_b128 v[38:41], v52 offset:14336\n"
    "  s_waitcnt lgkmcnt(4)\n"
    "  v_mfma_f32_16x16x32_bf16 v[0:3], v[34:37], v[76:79], v[0:3]\n"
    "  v_cvt_pk_bf16_f32 v16, v16, v17\n"
    "  v_cvt_pk_bf16_f32 v17, v18, v19\n"
    "  ds_read2st64_b64 v[24:27], v47 offset0:0 offset1:2\n"
    "  v_mfma_f32_16x16x32_bf16 v[4:7], v[30:33], v[60:63], 0\n"
    "  ds_write_b64 v46, v[16:17] offset:16384\n"
    "  v_mfma_f32_16x16x32_bf16 v[4:7], v[34:37], v[80:83], v[4:7]\n"
    "  ds_read_b128 v[42:45], v52 offset:16384\n"
    "  v_mfma_f32_16x16x32_bf16 v[8:11], v[30:33], v[64:67], 0\n"
    "  v_mfma_f32_16x16x32_bf16 v[8:11], v[34:37], v[84:87], v[8:11]\n"
    "  v_mfma_f32_16x16x32_bf16 v[12:15], v[30:33], v[68:71], 0\n"
    "  s_waitcnt lgkmcnt(2)\n"
    "  global_store_dwordx4 v54, v[24:27], s[40:41] offset:64 sc1 nt\n"
    "  v_mfma_f32_16x16x32_bf16 v[12:15], v[34:37], v[88:91], v[12:15]\n"
    "  v_mfma_f32_16x16x32_bf16 v[16:19], v[30:33], v[72:75], 0\n"
    "  v_xor_b32 v53, 96, v53\n"
    "  v_xor_b32 v46, 96, v46\n"
    "  v_mfma_f32_16x16x32_bf16 v[16:19], v[34:37], v[92:95], v[16:19]\n"
    "  ds_read_b128 v[30:33], v52 offset:11264\n"
    "  v_mfma_f32_16x16x32_bf16 v[0:3], v[38:41], v[96:99], v[0:3]\n"
    "  ds_read_b128 v[34:37], v52 offset:13312\n"
    "  v_mfma_f32_16x16x32_bf16 v[4:7], v[38:41], v[100:103], v[4:7]\n"
    "  ds_read_b64_tr_b16 v[20:21], v53 offset:2048\n"
    "  v_mfma_f32_16x16x32_bf16 v[8:11], v[38:41], v[104:107], v[8:11]\n"
    "  ds_read_b64_tr_b16 v[22:23], v53 offset:6144\n"
    "  v_mfma_f32_16x16x32_bf16 v[12:15], v[38:41], v[108:111], v[12:15]\n"
    "  ds_read_b64_tr_b16 v[24:25], v53 offset:10240\n"
    "  v_mfma_f32_16x16x32_bf16 v[16:19], v[38:41], v[112:115], v[16:19]\n"
    "  ds_read_b128 v[38:41], v52 offset:15360\n"
    "  s_waitcnt lgkmcnt(6)\n"
    "  v_mfma_f32_16x16x32_bf16 v[0:3], v[42:45], v[116:119], v[0:3]\n"
    "  ds_read_b64_tr_b16 v[26:27], v53 offset:14336\n"
    "  v_mfma_f32_16x16x32_bf16 v[4:7], v[42:45], v[120:123], v[4:7]\n"
    "  ds_read_b64_tr_b16 v[28:29], v53 offset:18432\n"
    "  v_mfma_f32_16x16x32_bf16 v[8:11], v[42:45], v[124:127], v[8:11]\n"
    "  v_mfma_f32_16x16x32_bf16 v[12:15], v[42:45], v[128:131], v[12:15]\n"
    "  v_mfma_f32_16x16x32_bf16 v[16:19], v[42:45], v[132:135], v[16:19]\n"
    "  ds_read_b128 v[42:45], v52 offset:17408\n"
    "  s_waitcnt lgkmcnt(8)\n"
    "  v_mfma_f32_16x16x32_bf16 v[0:3], v[30:33], v[136:139], v[0:3]\n"
    "  v_mfma_f32_16x16x32_bf16 v[4:7], v[30:33], v[140:143], v[4:7]\n"
    "  v_mfma_f32_16x16x32_bf16 v[8:11], v[30:33], v[144:147], v[8:11]\n"
    "  v_mfma_f32_16x16x32_bf16 v[12:15], v[30:33], v[148:151], v[12:15]\n"
    "  v_mfma_f32_16x16x32_bf16 v[16:19], v[30:33], v[152:155], v[16:19]\n"
    "  ds_read_b128 v[30:33], v52 offset:18432\n"
    "  s_waitcnt lgkmcnt(7)\n"
    "  v_mfma_f32_16x16x16_bf16 v[0:3], v[20:21], v[50:51], v[0:3]\n"
    "  s_waitcnt lgkmcnt(6)\n"
    "  v_mfma_f32_16x16x16_bf16 v[4:7], v[22:23], v[50:51], v[4:7]\n"
    "  s_waitcnt lgkmcnt(5)\n"
    "  v_mfma_f32_16x16x16_bf16 v[8:11], v[24:25], v[50:51], v[8:11]\n"
    "  s_waitcnt lgkmcnt(3)\n"
    "  v_mfma_f32_16x16x16_bf16 v[12:15], v[26:27], v[50:51], v[12:15]\n"
    "  s_waitcnt lgkmcnt(2)\n"
    "  v_mfma_f32_16x16x16_bf16 v[16:19], v[28:29], v[50:51], v[16:19]\n"
    "  v_mfma_f32_16x16x32_bf16 v[0:3], v[34:37], v[156:159], v[0:3]\n"
    "  ds_read2st64_b64 v[20:23], v47 offset0:8 offset1:10\n"
    "  v_mfma_f32_16x16x32_bf16 v[4:7], v[34:37], v[160:163], v[4:7]\n"
    "  v_mfma_f32_16x16x32_bf16 v[8:11], v[34:37], v[164:167], v[8:11]\n"
    "  v_mfma_f32_16x16x32_bf16 v[12:15], v[34:37], v[168:171], v[12:15]\n"
    "  v_mfma_f32_16x16x32_bf16 v[16:19], v[34:37], v[172:175], v[16:19]\n"
    "  ds_read_b128 v[34:37], v52 offset:19456\n"
    "  v_mfma_f32_16x16x32_bf16 v[0:3], v[38:41], v[176:179], v[0:3]\n"
    "  s_waitcnt lgkmcnt(1)\n"
    "  global_store_dwordx4 v54, v[20:23], s[42:43] offset:64 sc1 nt\n"
    "  v_mfma_f32_16x16x32_bf16 v[4:7], v[38:41], v[180:183], v[4:7]\n"
    "  v_mfma_f32_16x16x32_bf16 v[8:11], v[38:41], v[184:187], v[8:11]\n"
    "  v_mfma_f32_16x16x32_bf16 v[12:15], v[38:41], v[188:191], v[12:15]\n"
    "  v_mfma_f32_16x16x32_bf16 v[16:19], v[38:41], v[192:195], v[16:19]\n"
    "  v_mfma_f32_16x16x32_bf16 v[0:3], v[42:45], v[196:199], v[0:3]\n"
    "  v_mfma_f32_16x16x32_bf16 v[4:7], v[42:45], v[200:203], v[4:7]\n"
    "  v_mfma_f32_16x16x32_bf16 v[8:11], v[42:45], v[204:207], v[8:11]\n"
    "  v_mfma_f32_16x16x32_bf16 v[12:15], v[42:45], v[208:211], v[12:15]\n"
    "  v_mfma_f32_16x16x32_bf16 v[16:19], v[42:45], v[212:215], v[16:19]\n"
    "  v_mfma_f32_16x16x32_bf16 v[0:3], v[30:33], v[216:219], v[0:3]\n"
    "  s_waitcnt lgkmcnt(0)\n"
    "  v_mfma_f32_16x16x32_bf16 v[0:3], v[34:37], v[236:239], v[0:3]\n"
    "  ds_read_b32 v55, v48 offset:0\n"
    "  v_mfma_f32_16x16x32_bf16 v[4:7], v[30:33], v[220:223], v[4:7]\n"
    "  ds_read2st64_b64 v[24:27], v47 offset0:16 offset1:18\n"
    "  v_mfma_f32_16x16x32_bf16 v[4:7], v[34:37], v[240:243], v[4:7]\n"
    "  v_cvt_pk_bf16_f32 v0, v0, v1\n"
    "  v_cvt_pk_bf16_f32 v1, v2, v3\n"
    "  v_mfma_f32_16x16x32_bf16 v[8:11], v[30:33], v[224:227], v[8:11]\n"
    "  s_waitcnt lgkmcnt(1)\n"
    "  v_cmp_gt_u32_e32 vcc, 26, v55\n"
    "  s_cbranch_vccnz .Ledm_sl29\n"
    ".Ledm_bk30:\n"
    "  ds_read_b128 v[38:41], v52 offset:20480\n"
    "  ds_write_b64 v46, v[0:1] offset:2048\n"
    "  v_mfma_f32_16x16x32_bf16 v[8:11], v[34:37], v[244:247], v[8:11]\n"
    "  ds_read_b128 v[42:45], v52 offset:22528\n"
    "  v_cvt_pk_bf16_f32 v4, v4, v5\n"
    "  v_cvt_pk_bf16_f32 v5, v6, v7\n"
    "  v_mfma_f32_16x16x32_bf16 v[12:15], v[30:33], v[228:231], v[12:15]\n"
    "  ds_write_b64 v46, v[4:5] offset:6144\n"
    "  v_mfma_f32_16x16x32_bf16 v[12:15], v[34:37], v[248:251], v[12:15]\n"
    "  s_waitcnt lgkmcnt(4)\n"
    "  global_store_dwordx4 v54, v[24:27], s[44:45] offset:64 sc1 nt\n"
    "  v_cvt_pk_bf16_f32 v8, v8, v9\n"
    "  v_cvt_pk_bf16_f32 v9, v10, v11\n"
    "  v_mfma_f32_16x16x32_bf16 v[16:19], v[30:33], v[232:235], v[16:19]\n"
    "  ds_write_b64 v46, v[8:9] offset:10240\n"
    "  v_mfma_f32_16x16x32_bf16 v[16:19], v[34:37], v[252:255], v[16:19]\n"
    "  v_cvt_pk_bf16_f32 v12, v12, v13\n"
    "  v_cvt_pk_bf16_f32 v13, v14, v15\n"
    "  s_waitcnt lgkmcnt(4)\n"
    "  v_mfma_f32_16x16x32_bf16 v[0:3], v[38:41], v[56:59], 0\n"
    "  ds_write_b64 v46, v[12:13] offset:14336\n"
    "  ds_read_b128 v[30:33], v52 offset:24576\n"
    "  s_waitcnt lgkmcnt(4)\n"
    "  v_mfma_f32_16x16x32_bf16 v[0:3], v[42:45], v[76:79], v[0:3]\n"
    "  v_cvt_pk_bf16_f32 v16, v16, v17\n"
    "  v_cvt_pk_bf16_f32 v17, v18, v19\n"
    "  v_mfma_f32_16x16x32_bf16 v[4:7], v[38:41], v[60:63], 0\n"
    "  ds_write_b64 v46, v[16:17] offset:18432\n"
    "  v_mfma_f32_16x16x32_bf16 v[4:7], v[42:45], v[80:83], v[4:7]\n"
    "  ds_read_b128 v[34:37], v52 offset:26624\n"
    "  v_mfma_f32_16x16x32_bf16 v[8:11], v[38:41], v[64:67], 0\n"
    "  v_mfma_f32_16x16x32_bf16 v[8:11], v[42:45], v[84:87], v[8:11]\n"
    "  ds_read2st64_b64 v[20:23], v47 offset0:24 offset1:26\n"
    "  v_mfma_f32_16x16x32_bf16 v[12:15], v[38:41], v[68:71], 0\n"
    "  v_mfma_f32_16x16x32_bf16 v[12:15], v[42:45], v[88:91], v[12:15]\n"
    "  v_mfma_f32_16x16x32_bf16 v[16:19], v[38:41], v[72:75], 0\n"
    "  v_xor_b32 v53, 32, v53\n"
    "  v_xor_b32 v46, 32, v46\n"
    "  v_mfma_f32_16x16x32_bf16 v[16:19], v[42:45], v[92:95], v[16:19]\n"
    "  ds_read_b128 v[38:41], v52 offset:21504\n"
    "  s_waitcnt lgkmcnt(4)\n"
    "  v_mfma_f32_16x16x32_bf16 v[0:3], v[30:33], v[96:99], v[0:3]\n"
    "  s_waitcnt lgkmcnt(1)\n"
    "  global_store_dwordx4 v54, v[20:23], s[46:47] offset:64 sc1 nt\n"
    "  ds_read_b128 v[42:45], v52 offset:23552\n"
    "  v_mfma_f32_16x16x32_bf16 v[4:7], v[30:33], v[100:103], v[4:7]\n"
    "  ds_read_b64_tr_b16 v[20:21], v53 offset:2048\n"
    "  v_mfma_f32_16x16x32_bf16 v[8:11], v[30:33], v[104:107], v[8:11]\n"
    "  ds_read_b64_tr_b16 v[22:23], v53 offset:6144\n"
    "  v_mfma_f32_16x16x32_bf16 v[12:15], v[30:33], v[108:111], v[12:15]\n"
    "  ds_read_b64_tr_b16 v[24:25], v53 offset:10240\n"
    "  v_mfma_f32_16x16x32_bf16 v[16:19], v[30:33], v[112:115], v[16:19]\n"
    "  ds_read_b128 v[30:33], v52 offset:25600\n"
    "  v_mfma_f32_16x16x32_bf16 v[0:3], v[34:37], v[116:119], v[0:3]\n"
    "  ds_read_b64_tr_b16 v[26:27], v53 offset:14336\n"
    "  v_mfma_f32_16x16x32_bf16 v[4:7], v[34:37], v[120:123], v[4:7]\n"
    "  ds_read_b64_tr_b16 v[28:29], v53 offset:18432\n"
    "  v_mfma_f32_16x16x32_bf16 v[8:11], v[34:37], v[124:127], v[8:11]\n"
    "  v_mfma_f32_16x16x32_bf16 v[12:15], v[34:37], v[128:131], v[12:15]\n"
    "  v_mfma_f32_16x16x32_bf16 v[16:19], v[34:37], v[132:135], v[16:19]\n"
    "  ds_read_b128 v[34:37], v52 offset:27648\n"
    "  s_waitcnt lgkmcnt(8)\n"
    "  v_mfma_f32_16x16x32_bf16 v[0:3], v[38:41], v[136:139], v[0:3]\n"
    "  v_mfma_f32_16x16x32_bf16 v[4:7], v[38:41], v[140:143], v[4:7]\n"
    "  v_mfma_f32_16x16x32_bf16 v[8:11], v[38:41], v[144:147], v[8:11]\n"
    "  v_mfma_f32_16x16x32_bf16 v[12:15], v[38:41], v[148:151], v[12:15]\n"
    "  v_mfma_f32_16x16x32_bf16 v[16:19], v[38:41], v[152:155], v[16:19]\n"
    "  ds_read_b128 v[38:41], v52 offset:28672\n"
    "  s_waitcnt lgkmcnt(7)\n"
    "  v_mfma_f32_16x16x16_bf16 v[0:3], v[20:21], v[50:51], v[0:3]\n"
    "  s_waitcnt lgkmcnt(6)\n"
    "  v_mfma_f32_16x16x16_bf16 v[4:7], v[22:23], v[50:51], v[4:7]\n"
    "  s_waitcnt lgkmcnt(5)\n"
    "  v_mfma_f32_16x16x16_bf16 v[8:11], v[24:25], v[50:51], v[8:11]\n"
    "  s_waitcnt lgkmcnt(3)\n"
    "  v_mfma_f32_16x16x16_bf16 v[12:15], v[26:27], v[50:51], v[12:15]\n"
    "  s_waitcnt lgkmcnt(2)\n"
    "  v_mfma_f32_16x16x16_bf16 v[16:19], v[28:29], v[50:51], v[16:19]\n"
    "  v_mfma_f32_16x16x32_bf16 v[0:3], v[42:45], v[156:159], v[0:3]\n"
    "  v_mfma_f32_16x16x32_bf16 v[4:7], v[42:45], v[160:163], v[4:7]\n"
    "  v_mfma_f32_16x16x32_bf16 v[8:11], v[42:45], v[164:167], v[8:11]\n"
    "  ds_read2st64_b64 v[24:27], v47 offset0:32 offset1:34\n"
    "  v_mfma_f32_16x16x32_bf16 v[12:15], v[42:45], v[168:171], v[12:15]\n"
    "  v_xor_b32 v47, 64, v47\n"
    "  v_mfma_f32_16x16x32_bf16 v[16:19], v[42:45], v[172:175], v[16:19]\n"
    "  ds_read_b128 v[42:45], v52 offset:29696\n"
    "  v_mfma_f32_16x16x32_bf16 v[0:3], v[30:33], v[176:179], v[0:3]\n"
    "  v_mfma_f32_16x16x32_bf16 v[4:7], v[30:33], v[180:183], v[4:7]\n"
    "  v_mfma_f32_16x16x32_bf16 v[8:11], v[30:33], v[184:187], v[8:11]\n"
    "  s_waitcnt lgkmcnt(1)\n"
    "  global_store_dwordx4 v54, v[24:27], s[48:49] offset:64 sc1 nt\n"
    "  v_mfma_f32_16x16x32_bf16 v[12:15], v[30:33], v[188:191], v[12:15]\n"
    "  v_mfma_f32_16x16x32_bf16 v[16:19], v[30:33], v[192:195], v[16:19]\n"
    "  v_mfma_f32_16x16x32_bf16 v[0:3], v[34:37], v[196:199], v[0:3]\n"
    "  v_mfma_f32_16x16x32_bf16 v[4:7], v[34:37], v[200:203], v[4:7]\n"
    "  v_mfma_f32_16x16x32_bf16 v[8:11], v[34:37], v[204:207], v[8:11]\n"
    "  v_mfma_f32_16x16x32_bf16 v[12:15], v[34:37], v[208:211], v[12:15]\n"
    "  v_mfma_f32_16x16x32_bf16 v[16:19], v[34:37], v[212:215], v[16:19]\n"
    "  v_mfma_f32_16x16x32_bf16 v[0:3], v[38:41], v[216:219], v[0:3]\n"
    "  s_waitcnt lgkmcnt(0)\n"
    "  v_mfma_f32_16x16x32_bf16 v[0:3], v[42:45], v[236:239], v[0:3]\n"
    "  ds_read_b32 v55, v48 offset:0\n"
    "  v_mfma_f32_16x16x32_bf16 v[4:7], v[38:41], v[220:223], v[4:7]\n"
    "  v_mfma_f32_16x16x32_bf16 v[4:7], v[42:45], v[240:243], v[4:7]\n"
    "  v_cvt_pk_bf16_f32 v0, v0, v1\n"
    "  v_cvt_pk_bf16_f32 v1, v2, v3\n"
    "  v_mfma_f32_16x16x32_bf16 v[8:11], v[38:41], v[224:227], v[8:11]\n"
    "  s_waitcnt lgkmcnt(0)\n"
    "  v_cmp_gt_u32_e32 vcc, 27, v55\n"
    "  s_cbranch_vccnz .Ledm_sl31\n"
    ".Ledm_bk32:\n"
    "  ds_read_b128 v[30:33], v52 offset:30720\n"
    "  ds_write_b64 v46, v[0:1] offset:2048\n"
    "  v_mfma_f32_16x16x32_bf16 v[8:11], v[42:45], v[244:247], v[8:11]\n"
    "  ds_read_b128 v[34:37], v52 offset:32768\n"
    "  v_cvt_pk_bf16_f32 v4, v4, v5\n"
    "  v_cvt_pk_bf16_f32 v5, v6, v7\n"
    "  v_mfma_f32_16x16x32_bf16 v[12:15], v[38:41], v[228:231], v[12:15]\n"
    "  ds_write_b64 v46, v[4:5] offset:6144\n"
    "  v_mfma_f32_16x16x32_bf16 v[12:15], v[42:45], v[248:251], v[12:15]\n"
    "  v_cvt_pk_bf16_f32 v8, v8, v9\n"
    "  v_cvt_pk_bf16_f32 v9, v10, v11\n"
    "  v_mfma_f32_16x16x32_bf16 v[16:19], v[38:41], v[232:235], v[16:19]\n"
    "  ds_write_b64 v46, v[8:9] offset:10240\n"
    "  v_mfma_f32_16x16x32_bf16 v[16:19], v[42:45], v[252:255], v[16:19]\n"
    "  v_cvt_pk_bf16_f32 v12, v12, v13\n"
    "  v_cvt_pk_bf16_f32 v13, v14, v15\n"
    "  ds_read_b128 v[38:41], v52 offset:34816\n"
    "  s_waitcnt lgkmcnt(5)\n"
    "  v_mfma_f32_16x16x32_bf16 v[0:3], v[30:33], v[56:59], 0\n"
    "  ds_write_b64 v46, v[12:13] offset:14336\n"
    "  ds_read_b128 v[42:45], v52 offset:36864\n"
    "  s_waitcnt lgkmcnt(5)\n"
    "  v_mfma_f32_16x16x32_bf16 v[0:3], v[34:37], v[76:79], v[0:3]\n"
    "  v_cvt_pk_bf16_f32 v16, v16, v17\n"
    "  v_cvt_pk_bf16_f32 v17, v18, v19\n"
    "  v_mfma_f32_16x16x32_bf16 v[4:7], v[30:33], v[60:63], 0\n"
    "  ds_write_b64 v46, v[16:17] offset:18432\n"
    "  v_mfma_f32_16x16x32_bf16 v[4:7], v[34:37], v[80:83], v[4:7]\n"
    "  v_xor_b32 v53, 96, v53\n"
    "  v_xor_b32 v46, 96, v46\n"
    "  v_mfma_f32_16x16x32_bf16 v[8:11], v[30:33], v[64:67], 0\n"
    "  ds_read_b128 v[30:33], v52 offset:31744\n"
    "  ds_read_b64_tr_b16 v[20:21], v53 offset:2048\n"
    "  ds_read2st64_b64 v[12:15], v47 offset0:4 offset1:6\n"
    "  v_mfma_f32_16x16x32_bf16 v[8:11], v[34:37], v[84:87], v[8:11]\n"
    "  ds_read_b128 v[34:37], v52 offset:33792\n"
    "  ds_read_b64_tr_b16 v[22:23], v53 offset:6144\n"
    "  s_waitcnt lgkmcnt(8)\n"
    "  v_mfma_f32_16x16x32_bf16 v[0:3], v[38:41], v[96:99], v[0:3]\n"
    "  ds_read_b64_tr_b16 v[24:25], v53 offset:10240\n"
    "  v_mfma_f32_16x16x32_bf16 v[4:7], v[38:41], v[100:103], v[4:7]\n"
    "  ds_read_b64_tr_b16 v[26:27], v53 offset:14336\n"
    "  v_mfma_f32_16x16x32_bf16 v[8:11], v[38:41], v[104:107], v[8:11]\n"
    "  ds_read_b128 v[38:41], v52 offset:35840\n"
    "  ds_read_b64_tr_b16 v[28:29], v53 offset:18432\n"
    "  s_waitcnt lgkmcnt(10)\n"
    "  v_mfma_f32_16x16x32_bf16 v[0:3], v[42:45], v[116:119], v[0:3]\n"
    "  s_waitcnt lgkmcnt(6)\n"
    "  global_store_dwordx4 v54, v[12:15], s[40:41] offset:128 sc1 nt\n"
    "  v_mfma_f32_16x16x32_bf16 v[4:7], v[42:45], v[120:123], v[4:7]\n"
    "  ds_read2st64_b64 v[16:19], v47 offset0:12 offset1:14\n"
    "  v_mfma_f32_16x16x32_bf16 v[8:11], v[42:45], v[124:127], v[8:11]\n"
    "  ds_read_b128 v[42:45], v52 offset:37888\n"
    "  v_mfma_f32_16x16x32_bf16 v[0:3], v[30:33], v[136:139], v[0:3]\n"
    "  v_mfma_f32_16x16x32_bf16 v[4:7], v[30:33], v[140:143], v[4:7]\n"
    "  v_mfma_f32_16x16x32_bf16 v[8:11], v[30:33], v[144:147], v[8:11]\n"
    "  ds_read_b128 v[30:33], v52 offset:38912\n"
    "  v_mfma_f32_16x16x16_bf16 v[0:3], v[20:21], v[50:51], v[0:3]\n"
    "  s_waitcnt lgkmcnt(2)\n"
    "  global_store_dwordx4 v54, v[16:19], s[42:43] offset:128 sc1 nt\n"
    "  v_mfma_f32_16x16x16_bf16 v[4:7], v[22:23], v[50:51], v[4:7]\n"
    "  ds_read2st64_b64 v[12:15], v47 offset0:20 offset1:22\n"
    "  v_mfma_f32_16x16x16_bf16 v[8:11], v[24:25], v[50:51], v[8:11]\n"
    "  v_mfma_f32_16x16x32_bf16 v[0:3], v[34:37], v[156:159], v[0:3]\n"
    "  v_mfma_f32_16x16x32_bf16 v[4:7], v[34:37], v[160:163], v[4:7]\n"
    "  v_mfma_f32_16x16x32_bf16 v[8:11], v[34:37], v[164:167], v[8:11]\n"
    "  ds_read_b128 v[34:37], v52 offset:39936\n"
    "  v_mfma_f32_16x16x32_bf16 v[0:3], v[38:41], v[176:179], v[0:3]\n"
    "  ds_read_b32 v55, v48 offset:0\n"
    "  s_waitcnt lgkmcnt(2)\n"
    "  global_store_dwordx4 v54, v[12:15], s[44:45] offset:128 sc1 nt\n"
    "  v_mfma_f32_16x16x32_bf16 v[4:7], v[38:41], v[180:183], v[4:7]\n"
    "  ds_read2st64_b64 v[16:19], v47 offset0:28 offset1:30\n"
    "  v_mfma_f32_16x16x32_bf16 v[8:11], v[38:41], v[184:187], v[8:11]\n"
    "  v_mfma_f32_16x16x32_bf16 v[0:3], v[42:45], v[196:199], v[0:3]\n"
    "  s_waitcnt lgkmcnt(1)\n"
    "  v_cmp_gt_u32_e32 vcc, 28, v55\n"
    "  s_cbranch_vccnz .Ledm_sl33\n"
    ".Ledm_bk34:\n"
    "  ds_read_b128 v[38:41], v52 offset:40960\n"
    "  v_mfma_f32_16x16x32_bf16 v[4:7], v[42:45], v[200:203], v[4:7]\n"
    "  v_mfma_f32_16x16x32_bf16 v[8:11], v[42:45], v[204:207], v[8:11]\n"
    "  ds_read_b128 v[42:45], v52 offset:43008\n"
    "  v_mfma_f32_16x16x32_bf16 v[0:3], v[30:33], v[216:219], v[0:3]\n"
    "  s_waitcnt lgkmcnt(2)\n"
    "  global_store_dwordx4 v54, v[16:19], s[46:47] offset:128 sc1 nt\n"
    "  v_mfma_f32_16x16x32_bf16 v[0:3], v[34:37], v[236:239], v[0:3]\n"
    "  v_mfma_f32_16x16x32_bf16 v[4:7], v[30:33], v[220:223], v[4:7]\n"
    "  v_mfma_f32_16x16x32_bf16 v[4:7], v[34:37], v[240:243], v[4:7]\n"
    "  v_cvt_pk_bf16_f32 v0, v0, v1\n"
    "  v_cvt_pk_bf16_f32 v1, v2, v3\n"
    "  v_mfma_f32_16x16x32_bf16 v[8:11], v[30:33], v[224:227], v[8:11]\n"
    "  ds_read_b128 v[30:33], v52 offset:45056\n"
    "  ds_write_b64 v46, v[0:1] offset:2048\n"
    "  v_mfma_f32_16x16x32_bf16 v[8:11], v[34:37], v[244:247], v[8:11]\n"
    "  ds_read_b128 v[34:37], v52 offset:47104\n"
    "  v_cvt_pk_bf16_f32 v4, v4, v5\n"
    "  v_cvt_pk_bf16_f32 v5, v6, v7\n"
    "  s_waitcnt lgkmcnt(4)\n"
    "  v_mfma_f32_16x16x32_bf16 v[12:15], v[38:41], v[56:59], 0\n"
    "  ds_read_b128 v[56:59], v52 offset:41984\n"
    "  ds_write_b64 v46, v[4:5] offset:6144\n"
    "  s_waitcnt lgkmcnt(5)\n"
    "  v_mfma_f32_16x16x32_bf16 v[12:15], v[42:45], v[76:79], v[12:15]\n"
    "  ds_read_b128 v[76:79], v52 offset:44032\n"
    "  v_cvt_pk_bf16_f32 v8, v8, v9\n"
    "  v_cvt_pk_bf16_f32 v9, v10, v11\n"
    "  v_mfma_f32_16x16x32_bf16 v[16:19], v[38:41], v[60:63], 0\n"
    "  ds_write_b64 v46, v[8:9] offset:10240\n"
    "  v_mfma_f32_16x16x32_bf16 v[16:19], v[42:45], v[80:83], v[16:19]\n"
    "  v_mfma_f32_16x16x32_bf16 v[0:3], v[38:41], v[64:67], 0\n"
    "  v_mfma_f32_16x16x32_bf16 v[0:3], v[42:45], v[84:87], v[0:3]\n"
    "  s_waitcnt lgkmcnt(6)\n"
    "  v_mfma_f32_16x16x32_bf16 v[12:15], v[30:33], v[96:99], v[12:15]\n"
    "  ds_read_b128 v[96:99], v52 offset:46080\n"
    "  v_mov_b32 v64, v46\n"
    "  v_xor_b32 v53, 32, v53\n"
    "  v_xor_b32 v46, 32, v46\n"
    "  v_mfma_f32_16x16x32_bf16 v[16:19], v[30:33], v[100:103], v[16:19]\n"
    "  ds_read_b64_tr_b16 v[20:21], v53 offset:2048\n"
    "  ds_read2st64_b64 v[4:7], v47 offset0:36 offset1:38\n"
    "  v_mfma_f32_16x16x32_bf16 v[0:3], v[30:33], v[104:107], v[0:3]\n"
    "  ds_read_b64_tr_b16 v[22:23], v53 offset:6144\n"
    "  v_xor_b32 v47, 64, v47\n"
    "  s_waitcnt lgkmcnt(8)\n"
    "  v_mfma_f32_16x16x32_bf16 v[12:15], v[34:37], v[116:119], v[12:15]\n"
    "  ds_read_b128 v[116:119], v52 offset:48128\n"
    "  ds_read_b64_tr_b16 v[24:25], v53 offset:10240\n"
    "  v_mfma_f32_16x16x32_bf16 v[16:19], v[34:37], v[120:123], v[16:19]\n"
    "  ds_read_b64_tr_b16 v[84:85], v53 offset:14336\n"
    "  v_mfma_f32_16x16x32_bf16 v[0:3], v[34:37], v[124:127], v[0:3]\n"
    "  ds_read_b64_tr_b16 v[86:87], v53 offset:18432\n"
    "  s_waitcnt lgkmcnt(11)\n"
    "  v_mfma_f32_16x16x32_bf16 v[12:15], v[56:59], v[136:139], v[12:15]\n"
    "  ds_read_b128 v[136:139], v52 offset:49152\n"
    "  s_waitcnt lgkmcnt(6)\n"
    "  global_store_dwordx4 v54, v[4:7], s[48:49] offset:128 sc1 nt\n"
    "  v_mfma_f32_16x16x32_bf16 v[16:19], v[56:59], v[140:143], v[16:19]\n"
    "  v_mfma_f32_16x16x32_bf16 v[0:3], v[56:59], v[144:147], v[0:3]\n"
    "  v_mfma_f32_16x16x32_bf16 v[12:15], v[76:79], v[156:159], v[12:15]\n"
    "  ds_read_b128 v[156:159], v52 offset:50176\n"
    "  v_mfma_f32_16x16x32_bf16 v[16:19], v[76:79], v[160:163], v[16:19]\n"
    "  v_mfma_f32_16x16x32_bf16 v[0:3], v[76:79], v[164:167], v[0:3]\n"
    "  ds_read_b128 v[60:63], v52 offset:30720\n"
    "  v_mfma_f32_16x16x32_bf16 v[12:15], v[96:99], v[176:179], v[12:15]\n"
    "  v_mfma_f32_16x16x32_bf16 v[16:19], v[96:99], v[180:183], v[16:19]\n"
    "  ds_read_b128 v[80:83], v52 offset:32768\n"
    "  v_mfma_f32_16x16x32_bf16 v[0:3], v[96:99], v[184:187], v[0:3]\n"
    "  v_mfma_f32_16x16x16_bf16 v[12:15], v[20:21], v[50:51], v[12:15]\n"
    "  ds_read_b128 v[100:103], v52 offset:34816\n"
    "  s_waitcnt lgkmcnt(9)\n"
    "  v_mfma_f32_16x16x16_bf16 v[16:19], v[22:23], v[50:51], v[16:19]\n"
    "  s_waitcnt lgkmcnt(7)\n"
    "  v_mfma_f32_16x16x16_bf16 v[0:3], v[24:25], v[50:51], v[0:3]\n"
    "  ds_read_b128 v[120:123], v52 offset:36864\n"
    "  v_mfma_f32_16x16x32_bf16 v[12:15], v[116:119], v[196:199], v[12:15]\n"
    "  v_mfma_f32_16x16x32_bf16 v[16:19], v[116:119], v[200:203], v[16:19]\n"
    "  ds_read_b128 v[140:143], v52 offset:31744\n"
    "  v_mfma_f32_16x16x32_bf16 v[0:3], v[116:119], v[204:207], v[0:3]\n"
    "  s_waitcnt lgkmcnt(6)\n"
    "  v_mfma_f32_16x16x32_bf16 v[12:15], v[136:139], v[216:219], v[12:15]\n"
    "  ds_read_b128 v[160:163], v52 offset:33792\n"
    "  s_waitcnt lgkmcnt(6)\n"
    "  v_mfma_f32_16x16x32_bf16 v[12:15], v[156:159], v[236:239], v[12:15]\n"
    "  v_mfma_f32_16x16x32_bf16 v[16:19], v[136:139], v[220:223], v[16:19]\n"
    "  ds_read_b128 v[180:183], v52 offset:35840\n"
    "  v_mfma_f32_16x16x32_bf16 v[16:19], v[156:159], v[240:243], v[16:19]\n"
    "  v_cvt_pk_bf16_f32 v12, v12, v13\n"
    "  v_cvt_pk_bf16_f32 v13, v14, v15\n"
    "  v_mfma_f32_16x16x32_bf16 v[0:3], v[136:139], v[224:227], v[0:3]\n"
    "  ds_read_b128 v[200:203], v52 offset:37888\n"
    "  ds_write_b64 v46, v[12:13] offset:2048\n"
    "  v_mfma_f32_16x16x32_bf16 v[0:3], v[156:159], v[244:247], v[0:3]\n"
    "  v_cvt_pk_bf16_f32 v16, v16, v17\n"
    "  v_cvt_pk_bf16_f32 v17, v18, v19\n"
    "  v_mfma_f32_16x16x32_bf16 v[4:7], v[38:41], v[68:71], 0\n"
    "  ds_read_b128 v[220:223], v52 offset:38912\n"
    "  ds_write_b64 v46, v[16:17] offset:6144\n"
    "  v_mfma_f32_16x16x32_bf16 v[4:7], v[42:45], v[88:91], v[4:7]\n"
    "  v_cvt_pk_bf16_f32 v0, v0, v1\n"
    "  v_cvt_pk_bf16_f32 v1, v2, v3\n"
    "  s_waitcnt lgkmcnt(10)\n"
    "  v_mfma_f32_16x16x32_bf16 v[8:11], v[60:63], v[68:71], 0\n"
    "  ds_read_b128 v[240:243], v52 offset:39936\n"
    "  ds_write_b64 v46, v[0:1] offset:10240\n"
    "  ds_read2st64_b64 v[104:107], v47 offset0:4 offset1:6\n"
    "  s_waitcnt lgkmcnt(12)\n"
    "  v_mfma_f32_16x16x32_bf16 v[8:11], v[80:83], v[88:91], v[8:11]\n"
    "  v_mfma_f32_16x16x32_bf16 v[4:7], v[30:33], v[108:111], v[4:7]\n"
    "  v_mfma_f32_16x16x32_bf16 v[4:7], v[34:37], v[128:131], v[4:7]\n"
    "  ds_read2st64_b64 v[124:127], v47 offset0:12 offset1:14\n"
    "  s_waitcnt lgkmcnt(12)\n"
    "  v_mfma_f32_16x16x32_bf16 v[8:11], v[100:103], v[108:111], v[8:11]\n"
    "  s_waitcnt lgkmcnt(11)\n"
    "  v_mfma_f32_16x16x32_bf16 v[8:11], v[120:123], v[128:131], v[8:11]\n"
    "  s_waitcnt lgkmcnt(1)\n"
    "  global_store_dwordx4 v54, v[104:107], s[40:41] offset:192 sc1 nt\n"
    "  v_mfma_f32_16x16x32_bf16 v[4:7], v[56:59], v[148:151], v[4:7]\n"
    "  ds_read2st64_b64 v[144:147], v47 offset0:20 offset1:22\n"
    "  v_mfma_f32_16x16x32_bf16 v[4:7], v[76:79], v[168:171], v[4:7]\n"
    "  v_mfma_f32_16x16x32_bf16 v[8:11], v[140:143], v[148:151], v[8:11]\n"
    "  s_waitcnt lgkmcnt(1)\n"
    "  global_store_dwordx4 v54, v[124:127], s[42:43] offset:192 sc1 nt\n"
    "  v_mfma_f32_16x16x32_bf16 v[8:11], v[160:163], v[168:171], v[8:11]\n"
    "  v_mfma_f32_16x16x16_bf16 v[4:7], v[84:85], v[50:51], v[4:7]\n"
    "  v_mfma_f32_16x16x16_bf16 v[8:11], v[26:27], v[50:51], v[8:11]\n"
    "  s_waitcnt lgkmcnt(0)\n"
    "  global_store_dwordx4 v54, v[144:147], s[44:45] offset:192 sc1 nt\n"
    "  v_mfma_f32_16x16x32_bf16 v[4:7], v[96:99], v[188:191], v[4:7]\n"
    "  v_mfma_f32_16x16x32_bf16 v[4:7], v[116:119], v[208:211], v[4:7]\n"
    "  v_mfma_f32_16x16x32_bf16 v[4:7], v[136:139], v[228:231], v[4:7]\n"
    "  v_mfma_f32_16x16x32_bf16 v[4:7], v[156:159], v[248:251], v[4:7]\n"
    "  v_mfma_f32_16x16x32_bf16 v[8:11], v[180:183], v[188:191], v[8:11]\n"
    "  v_mfma_f32_16x16x32_bf16 v[8:11], v[200:203], v[208:211], v[8:11]\n"
    "  v_cvt_pk_bf16_f32 v4, v4, v5\n"
    "  v_cvt_pk_bf16_f32 v5, v6, v7\n"
    "  ds_write_b64 v46, v[4:5] offset:14336\n"
    "  v_mfma_f32_16x16x32_bf16 v[8:11], v[220:223], v[228:231], v[8:11]\n"
    "  v_mfma_f32_16x16x32_bf16 v[8:11], v[240:243], v[248:251], v[8:11]\n"
    "  v_mfma_f32_16x16x32_bf16 v[12:15], v[38:41], v[72:75], 0\n"
    "  v_mfma_f32_16x16x32_bf16 v[12:15], v[42:45], v[92:95], v[12:15]\n"
    "  v_cvt_pk_bf16_f32 v8, v8, v9\n"
    "  v_cvt_pk_bf16_f32 v9, v10, v11\n"
    "  ds_write_b64 v64, v[8:9] offset:14336\n"
    "  ds_read2st64_b64 v[164:167], v47 offset0:28 offset1:30\n"
    "  v_mfma_f32_16x16x32_bf16 v[16:19], v[60:63], v[72:75], 0\n"
    "  v_mfma_f32_16x16x32_bf16 v[16:19], v[80:83], v[92:95], v[16:19]\n"
    "  v_mfma_f32_16x16x32_bf16 v[12:15], v[30:33], v[112:115], v[12:15]\n"
    "  v_mfma_f32_16x16x32_bf16 v[12:15], v[34:37], v[132:135], v[12:15]\n"
    "  v_mfma_f32_16x16x32_bf16 v[16:19], v[100:103], v[112:115], v[16:19]\n"
    "  s_waitcnt lgkmcnt(0)\n"
    "  global_store_dwordx4 v54, v[164:167], s[46:47] offset:192 sc1 nt\n"
    "  v_mfma_f32_16x16x32_bf16 v[16:19], v[120:123], v[132:135], v[16:19]\n"
    "  v_mfma_f32_16x16x32_bf16 v[12:15], v[56:59], v[152:155], v[12:15]\n"
    "  v_mfma_f32_16x16x32_bf16 v[12:15], v[76:79], v[172:175], v[12:15]\n"
    "  v_mfma_f32_16x16x32_bf16 v[16:19], v[140:143], v[152:155], v[16:19]\n"
    "  v_mfma_f32_16x16x32_bf16 v[16:19], v[160:163], v[172:175], v[16:19]\n"
    "  v_mfma_f32_16x16x16_bf16 v[12:15], v[86:87], v[50:51], v[12:15]\n"
    "  v_mfma_f32_16x16x16_bf16 v[16:19], v[28:29], v[50:51], v[16:19]\n"
    "  v_mfma_f32_16x16x32_bf16 v[12:15], v[96:99], v[192:195], v[12:15]\n"
    "  v_mfma_f32_16x16x32_bf16 v[12:15], v[116:119], v[212:215], v[12:15]\n"
    "  v_mfma_f32_16x16x32_bf16 v[12:15], v[136:139], v[232:235], v[12:15]\n"
    "  v_mfma_f32_16x16x32_bf16 v[12:15], v[156:159], v[252:255], v[12:15]\n"
    "  v_mfma_f32_16x16x32_bf16 v[16:19], v[180:183], v[192:195], v[16:19]\n"
    "  v_mfma_f32_16x16x32_bf16 v[16:19], v[200:203], v[212:215], v[16:19]\n"
    "  v_cvt_pk_bf16_f32 v12, v12, v13\n"
    "  v_cvt_pk_bf16_f32 v13, v14, v15\n"
    "  ds_write_b64 v46, v[12:13] offset:18432\n"
    "  v_mfma_f32_16x16x32_bf16 v[16:19], v[220:223], v[232:235], v[16:19]\n"
    "  v_mfma_f32_16x16x32_bf16 v[16:19], v[240:243], v[252:255], v[16:19]\n"
    "  s_nop 7\n"
    "  v_cvt_pk_bf16_f32 v16, v16, v17\n"
    "  v_cvt_pk_bf16_f32 v17, v18, v19\n"
    "  ds_write_b64 v64, v[16:17] offset:18432\n"
    "  ds_read2st64_b64 v[184:187], v47 offset0:36 offset1:38\n"
    "  s_waitcnt lgkmcnt(0)\n"
    "  global_store_dwordx4 v54, v[184:187], s[48:49] offset:192 sc1 nt\n"
    "  s_endpgm\n"
    ".Ledm_sl21:\n"
    "  s_sub_u32 s39, s39, 1\n"
    "  s_cbranch_scc1 .Ledm_abort\n"
    "  ds_read_b32 v55, v48 offset:0\n"
    "  s_waitcnt lgkmcnt(0)\n"
    "  v_cmp_gt_u32_e32 vcc, 22, v55\n"
    "  s_cbranch_vccnz .Ledm_sl21\n"
    "  s_branch .Ledm_bk22\n"
    ".Ledm_sl23:\n"
    "  s_sub_u32 s39, s39, 1\n"
    "  s_cbranch_scc1 .Ledm_abort\n"
    "  ds_read_b32 v55, v48 offset:0\n"
    "  s_waitcnt lgkmcnt(0)\n"
    "  v_cmp_gt_u32_e32 vcc, 23, v55\n"
    "  s_cbranch_vccnz .Ledm_sl23\n"
    "  s_branch .Ledm_bk24\n"
    ".Ledm_sl25:\n"
    "  s_sub_u32 s39, s39, 1\n"
    "  s_cbranch_scc1 .Ledm_abort\n"
    "  ds_read_b32 v55, v48 offset:0\n"
    "  s_waitcnt lgkmcnt(0)\n"
    "  v_cmp_gt_u32_e32 vcc, 24, v55\n"
    "  s_cbranch_vccnz .Ledm_sl25\n"
    "  s_branch .Ledm_bk26\n"
    ".Ledm_sl27:\n"
    "  s_sub_u32 s39, s39, 1\n"
    "  s_cbranch_scc1 .Ledm_abort\n"
    "  ds_read_b32 v55, v48 offset:0\n"
    "  s_waitcnt lgkmcnt(0)\n"
    "  v_cmp_gt_u32_e32 vcc, 25, v55\n"
    "  s_cbranch_vccnz .Ledm_sl27\n"
    "  s_branch .Ledm_bk28\n"
    ".Ledm_sl29:\n"
    "  s_sub_u32 s39, s39, 1\n"
    "  s_cbranch_scc1 .Ledm_abort\n"
    "  ds_read_b32 v55, v48 offset:0\n"
    "  s_waitcnt lgkmcnt(0)\n"
    "  v_cmp_gt_u32_e32 vcc, 26, v55\n"
    "  s_cbranch_vccnz .Ledm_sl29\n"
    "  s_branch .Ledm_bk30\n"
    ".Ledm_sl31:\n"
    "  s_sub_u32 s39, s39, 1\n"
    "  s_cbranch_scc1 .Ledm_abort\n"
    "  ds_read_b32 v55, v48 offset:0\n"
    "  s_waitcnt lgkmcnt(0)\n"
    "  v_cmp_gt_u32_e32 vcc, 27, v55\n"
    "  s_cbranch_vccnz .Ledm_sl31\n"
    "  s_branch .Ledm_bk32\n"
    ".Ledm_sl33:\n"
    "  s_sub_u32 s39, s39, 1\n"
    "  s_cbranch_scc1 .Ledm_abort\n"
    "  ds_read_b32 v55, v48 offset:0\n"
    "  s_waitcnt lgkmcnt(0)\n"
    "  v_cmp_gt_u32_e32 vcc, 28, v55\n"
    "  s_cbranch_vccnz .Ledm_sl33\n"
    "  s_branch .Ledm_bk34\n"
    ".Ledm_abort:\n"
    "  s_endpgm\n"
    ".Ledm_end:\n"
    :: "s"(ka), "s"(blk), "v"(tid), "s"((unsigned)(size_t)lds_image) : "v1", "v2", "v3", "v4", "v5", "v6", "v7", "v8", "v9", "v10", "v11", "v12", "v13", "v14", "v15", "v16", "v17", "v18", "v19", "v20", "v21", "v22", "v23", "v24", "v25", "v26", "v27", "v28", "v29", "v30", "v31", "v32", "v33", "v34", "v35", "v36", "v37", "v38", "v39", "v40", "v41", "v42", "v43", "v44", "v45", "v46", "v47", "v48", "v49", "v50", "v51", "v52", "v53", "v54", "v55", "v56", "v57", "v58", "v59", "v60", "v61", "v62", "v63", "v64", "v65", "v66", "v67", "v68", "v69", "v70", "v71", "v72", "v73", "v74", "v75", "v76", "v77", "v78", "v79", "v80", "v81", "v82", "v83", "v84", "v85", "v86", "v87", "v88", "v89", "v90", "v91", "v92", "v93", "v94", "v95", "v96", "v97", "v98", "v99", "v100", "v101", "v102", "v103", "v104", "v105", "v106", "v107", "v108", "v109", "v110", "v111", "v112", "v113", "v114", "v115", "v116", "v117", "v118", "v119", "v120", "v121", "v122", "v123", "v124", "v125", "v126", "v127", "v128", "v129", "v130", "v131", "v132", "v133", "v134", "v135", "v136", "v137", "v138", "v139", "v140", "v141", "v142", "v143", "v144", "v145", "v146", "v147", "v148", "v149", "v150", "v151", "v152", "v153", "v154", "v155", "v156", "v157", "v158", "v159", "v160", "v161", "v162", "v163", "v164", "v165", "v166", "v167", "v168", "v169", "v170", "v171", "v172", "v173", "v174", "v175", "v176", "v177", "v178", "v179", "v180", "v181", "v182", "v183", "v184", "v185", "v186", "v187", "v188", "v189", "v190", "v191", "v192", "v193", "v194", "v195", "v196", "v197", "v198", "v199", "v200", "v201", "v202", "v203", "v204", "v205", "v206", "v207", "v208", "v209", "v210", "v211", "v212", "v213", "v214", "v215", "v216", "v217", "v218", "v219", "v220", "v221", "v222", "v223", "v224", "v225", "v226", "v227", "v228", "v229", "v230", "v231", "v232", "v233", "v234", "v235", "v236", "v237", "v238", "v239", "v240", "v241", "v242", "v243", "v244", "v245", "v246", "v247", "v248", "v249", "v250", "v251", "v252", "v253", "v254", "v255", "s8", "s9", "s10", "s11", "s12", "s13", "s14", "s15", "s16", "s17", "s18", "s19", "s20", "s21", "s22", "s23", "s24", "s25", "s26", "s27", "s28", "s29", "s30", "s31", "s34", "s35", "s36", "s37", "s38", "s39", "s40", "s41", "s42", "s43", "s44", "s45", "s46", "s47", "s48", "s49", "s50", "s51", "s52", "s53", "s54", "s55", "s56", "s57", "s58", "s59", "s60", "s61", "s62", "s63", "s64", "s65", "s66", "s67", "s68", "s69", "s70", "s71", "s72", "s73", "s74", "s75", "s76", "s77", "s78", "s79", "s80", "s81", "s82", "s83", "s84", "s85", "s86", "s87", "s88", "s89", "s90", "s91", "s92", "s93", "s94", "s95", "vcc", "scc", "memory");
}

extern "C" void kernel_launch(void* const* d_in, const int* in_sizes, int n_in, void* d_out, int out_size, void* d_ws, size_t ws_size, hipStream_t stream) {
  edm_precond_kernel<<<256, 512, 0, stream>>>(d_in[0], d_in[1], d_in[2], d_in[3], d_in[4], d_out);
}
```
